```python
import torch

def _softmax(x, axis=-1):
    return torch.softmax(x, dim=axis)
def _gelu(x):
    return torch.nn.functional.gelu(x, approximate="tanh")
def _silu(x):
    return torch.nn.functional.silu(x)
def _sigmoid(x):
    return torch.sigmoid(x)
def _layernorm(x, axis=-1, eps=1e-5):
    m = x.mean(dim=axis, keepdim=True)
    v = x.var(dim=axis, keepdim=True, unbiased=False)
    return (x - m) / torch.sqrt(v + eps)
def _scatter_add(tgt, idx, src, axis=0):
    return tgt.clone().index_add_(axis, idx, src)
def _segment_sum(src, seg, n):
    out = torch.zeros((n,) + src.shape[1:], dtype=src.dtype)
    return out.index_add_(0, seg, src)
def _pad(x, pw):
    flat = tuple(v for pair in reversed(pw) for v in pair)
    return torch.nn.functional.pad(x, flat)

kernel_name = 'standalone__deep_cross_pnn__recsys'

def setup_inputs():

    _w = (lambda a: (lambda t: t.to(torch.bfloat16) if t.is_floating_point() else t)(torch.as_tensor(a)))
    B, F, D, V = 4096, 100, 128, min(1000, 100000)
    H = 4 * D
    return {
        "idx": _w((lambda lo, hi, *s: torch.randint(lo, hi, s))(0, V, B, F).to(torch.int64)),
        "table": _w((lambda *s: torch.randn(*s, dtype=torch.bfloat16) * 1.6)(V, D) * (D ** -0.5)),
        "w_p": _w((lambda *s: torch.randn(*s, dtype=torch.bfloat16) * 0.05)(F * F) * ((F * F) ** -0.5)),
        "w1": _w((lambda *s: torch.randn(*s, dtype=torch.bfloat16) * 1.6)(F * D, H) * ((F * D) ** -0.5)),
        "b1": _w((lambda *s: torch.randn(*s, dtype=torch.bfloat16) * 6.4)(H) * 0.01),
        "w2": _w((lambda *s: torch.randn(*s, dtype=torch.bfloat16) * 0.05)(H) * (H ** -0.5)),
        "b2": _w((lambda *s: torch.randn(*s, dtype=torch.bfloat16) * 0.8)(1) * 0.01),
    }

def reference(idx, table, w_p, w1, b1, w2, b2):
    emb = table[idx]
    B = emb.shape[0]; F = emb.shape[1]; D = emb.shape[2]
    ip = torch.einsum('bid,bjd->bij', emb, emb)
    ip = ip.reshape(B, F * F)
    logit_p = ip @ w_p
    z = emb.reshape(B, F * D)
    h = _gelu(z @ w1 + b1)
    logit_d = h @ w2 + b2
    return logit_p + logit_d
```

```cpp
#include <hip/hip_runtime.h>
#include <hip/hip_bf16.h>
#include <stdint.h>
#pragma clang diagnostic ignored "-Wint-to-pointer-cast"

typedef short bf16x8 __attribute__((ext_vector_type(8)));
typedef short bf16x4 __attribute__((ext_vector_type(4)));
typedef float f32x4 __attribute__((ext_vector_type(4)));
typedef float f32x2 __attribute__((ext_vector_type(2)));
typedef unsigned u32x4 __attribute__((ext_vector_type(4)));
typedef unsigned u32x2 __attribute__((ext_vector_type(2)));
typedef __bf16 bfx2 __attribute__((ext_vector_type(2)));
#define LDS_AS __attribute__((address_space(3)))

constexpr int B_ = 4096, F_ = 100, D_ = 128, V_ = 1000, H_ = 512;
constexpr int KD = F_ * D_;

constexpr size_t WS_WT = 0;
constexpr size_t WS_TAB = WS_WT + (size_t)H_ * KD * 2;
constexpr size_t WS_IDX = WS_TAB + 64 * 4 * 1024;
constexpr size_t WS_IDXS = WS_IDX + 3 * 8 * 32 * 1024;
constexpr size_t WS_LP = WS_IDXS + (size_t)B_ * 8;
constexpr size_t WS_PART = WS_LP + (size_t)B_ * 4;

__device__ __forceinline__ float bf2f(unsigned short u) { return __uint_as_float((unsigned)u << 16); }
__device__ __forceinline__ unsigned pack_bf16(float a, float b) {
  f32x2 v = {a, b};
  bfx2 r = __builtin_convertvector(v, bfx2);
  return __builtin_bit_cast(unsigned, r);
}
__device__ __forceinline__ float round_bf16(float a) {
  return __uint_as_float(pack_bf16(a, 0.f) << 16);
}

__global__ __launch_bounds__(512, 2) void k1_prep_pnn(const int* __restrict__ idx,
                                                      const unsigned short* __restrict__ table,
                                                      const unsigned short* __restrict__ w_p,
                                                      const unsigned short* __restrict__ w1,
                                                      char* __restrict__ ws) {
  __shared__ __attribute__((aligned(16))) char smem[28 * 1024 + 20480 + 6400];
  float* s_wimg = (float*)smem;
  unsigned short* s_wp = (unsigned short*)(smem + 28 * 1024);
  int* s_idx = (int*)(smem + 28 * 1024 + 20480);

  const int tid = threadIdx.x, lane = tid & 63;
  const int wave = __builtin_amdgcn_readfirstlane(tid >> 6);
  const int wg = blockIdx.x;
  const int b0 = wg * 16;

  u32x4 st_idx, st_wp[3];
  if (tid < 400) st_idx = *(const u32x4*)((const char*)(idx + (size_t)b0 * F_) + tid * 16);
#pragma unroll
  for (int i = 0; i < 3; ++i) {
    int c = tid + i * 512;
    if (c < 1250) st_wp[i] = *(const u32x4*)((const char*)w_p + c * 16);
  }
  const int unit = wave * 256 + wg;
  const bool has_unit = unit < 1600;
  u32x4 wr[8];
  const int ub_k = (unit >> 3) * 64 + (lane >> 3) * 8;
  const int ub_h = (unit & 7) * 64 + (lane & 7) * 8;
  if (has_unit) {
#pragma unroll
    for (int r = 0; r < 8; ++r) wr[r] = *(const u32x4*)(w1 + (size_t)(ub_k + r) * H_ + ub_h);
  }
  if (wave == 7) {
    const int vt = wg >> 2, s = wg & 3;
    const int v = vt * 16 + (lane & 15);
    u32x4 t = {0u, 0u, 0u, 0u};
    if (v < V_) t = *(const u32x4*)(table + (size_t)v * D_ + s * 32 + (lane >> 4) * 8);
    *(u32x4*)(ws + WS_TAB + (size_t)wg * 1024 + lane * 16) = t;
  }
  if (tid < 400) *(u32x4*)((char*)s_idx + tid * 16) = st_idx;
#pragma unroll
  for (int i = 0; i < 3; ++i) {
    int c = tid + i * 512;
    if (c < 1250) *(u32x4*)((char*)s_wp + c * 16) = st_wp[i];
  }
  __syncthreads();

  const char* tb = (const char*)table + (lane >> 4) * 16;
  bf16x8 ea[7][4], eb[7][4];
  {
    const int* ir = s_idx + (wave * 2) * F_;
#pragma unroll
    for (int I = 0; I < 7; ++I) {
      int f = I * 16 + (lane & 15);
      f = f < F_ ? f : F_ - 1;
      const char* p = tb + (size_t)ir[f] * 256;
#pragma unroll
      for (int s = 0; s < 4; ++s) ea[I][s] = *(const bf16x8*)(p + s * 64);
    }
  }
#pragma unroll
  for (int q = 0; q < 4; ++q) {
    const int t = wave + q * 8;
    if (t < 28) {
      int J = 0;
      while ((J + 1) * (J + 2) / 2 <= t) ++J;
      const int I = t - J * (J + 1) / 2;
      const int i0 = I * 16 + (lane >> 4) * 4, j = J * 16 + (lane & 15);
      f32x4 w = {0.f, 0.f, 0.f, 0.f};
      if (i0 < F_ && j < F_) {
#pragma unroll
        for (int r = 0; r < 4; ++r) w[r] = bf2f(s_wp[(i0 + r) * F_ + j]);
        if (I != J) {
          u32x2 x = *(const u32x2*)(s_wp + j * F_ + i0);
          w[0] += __uint_as_float(x[0] << 16);
          w[1] += __uint_as_float(x[0] & 0xffff0000u);
          w[2] += __uint_as_float(x[1] << 16);
          w[3] += __uint_as_float(x[1] & 0xffff0000u);
        }
      }
      *(f32x4*)(s_wimg + (t * 64 + lane) * 4) = w;
    }
  }
  if (tid < 208) {
    const int i = tid & 15, combo = tid >> 4;
    const int b = b0 + i, bl = b & 63;
    const int* ir = s_idx + i * F_;
    if (combo < 12) {
      const int p = combo >> 2, qd = combo & 3;
      unsigned o[8];
#pragma unroll
      for (int j = 0; j < 8; ++j) {
        const int slot = (bl + 8 * qd + j) & 31;
        o[j] = (unsigned)ir[32 * p + slot] * 32 + slot;
      }
      u32x4 r = {o[0] | (o[1] << 16), o[2] | (o[3] << 16), o[4] | (o[5] << 16), o[6] | (o[7] << 16)};
      *(u32x4*)(ws + WS_IDX + ((size_t)((p * 64 + (b >> 6)) * 4 + qd) * 64 + bl) * 16) = r;
    } else {
      unsigned o[4];
#pragma unroll
      for (int j = 0; j < 4; ++j) {
        const int slot = (bl + j) & 3;
        o[j] = 32768u + (unsigned)ir[96 + slot] * 4 + slot;
      }
      u32x2 r = {o[0] | (o[1] << 16), o[2] | (o[3] << 16)};
      *(u32x2*)(ws + WS_IDXS + (size_t)b * 8) = r;
    }
  }
  if (has_unit) {
    unsigned short* wt = (unsigned short*)(ws + WS_WT);
#pragma unroll
    for (int c = 0; c < 4; ++c) {
      u32x4 lo, hi;
#pragma unroll
      for (int r = 0; r < 4; ++r) {
        lo[r] = __builtin_amdgcn_perm(wr[2 * r + 1][c], wr[2 * r][c], 0x05040100u);
        hi[r] = __builtin_amdgcn_perm(wr[2 * r + 1][c], wr[2 * r][c], 0x07060302u);
      }
      *(u32x4*)(wt + (size_t)(ub_h + 2 * c) * KD + ub_k) = lo;
      *(u32x4*)(wt + (size_t)(ub_h + 2 * c + 1) * KD + ub_k) = hi;
    }
  }
  const int* irb = s_idx + (wave * 2 + 1) * F_;
  auto load_b = [&](int I) {
    int f = I * 16 + (lane & 15);
    f = f < F_ ? f : F_ - 1;
    const char* p = tb + (size_t)irb[f] * 256;
#pragma unroll
    for (int s = 0; s < 4; ++s) eb[I][s] = *(const bf16x8*)(p + s * 64);
  };
#pragma unroll
  for (int I = 0; I < 5; ++I) load_b(I);
  __syncthreads();

  float ra, rb;
  {
    float sum0 = 0.f, sum1 = 0.f;
    f32x4 dp = {0.f, 0.f, 0.f, 0.f}, wp = {0.f, 0.f, 0.f, 0.f};
#pragma unroll
    for (int I = 0; I < 7; ++I) {
#pragma unroll
      for (int J = I; J < 7; ++J) {
        const int t = J * (J + 1) / 2 + I;
        const f32x4 w = *(const f32x4*)(s_wimg + (t * 64 + lane) * 4);
        f32x4 d = {0.f, 0.f, 0.f, 0.f};
#pragma unroll
        for (int s = 0; s < 4; ++s) d = __builtin_amdgcn_mfma_f32_16x16x32_bf16(ea[I][s], ea[J][s], d, 0, 0, 0);
        sum0 = fmaf(dp[0], wp[0], sum0);
        sum1 = fmaf(dp[1], wp[1], sum1);
        sum0 = fmaf(dp[2], wp[2], sum0);
        sum1 = fmaf(dp[3], wp[3], sum1);
        dp = d;
        wp = w;
        asm volatile("" : "+v"(sum0), "+v"(sum1));
        __builtin_amdgcn_sched_barrier(0);
      }
      if (I == 0) load_b(5);
      if (I == 1) load_b(6);
    }
    sum0 = fmaf(dp[0], wp[0], sum0);
    sum1 = fmaf(dp[1], wp[1], sum1);
    sum0 = fmaf(dp[2], wp[2], sum0);
    sum1 = fmaf(dp[3], wp[3], sum1);
    ra = sum0 + sum1;
  }
  {
    float sum0 = 0.f, sum1 = 0.f;
    f32x4 dp = {0.f, 0.f, 0.f, 0.f}, wp = {0.f, 0.f, 0.f, 0.f};
#pragma unroll
    for (int J = 0; J < 7; ++J) {
#pragma unroll
      for (int I = 0; I <= J; ++I) {
        const int t = J * (J + 1) / 2 + I;
        const f32x4 w = *(const f32x4*)(s_wimg + (t * 64 + lane) * 4);
        f32x4 d = {0.f, 0.f, 0.f, 0.f};
#pragma unroll
        for (int s = 0; s < 4; ++s) d = __builtin_amdgcn_mfma_f32_16x16x32_bf16(eb[I][s], eb[J][s], d, 0, 0, 0);
        sum0 = fmaf(dp[0], wp[0], sum0);
        sum1 = fmaf(dp[1], wp[1], sum1);
        sum0 = fmaf(dp[2], wp[2], sum0);
        sum1 = fmaf(dp[3], wp[3], sum1);
        dp = d;
        wp = w;
        asm volatile("" : "+v"(sum0), "+v"(sum1));
        __builtin_amdgcn_sched_barrier(0);
      }
    }
    sum0 = fmaf(dp[0], wp[0], sum0);
    sum1 = fmaf(dp[1], wp[1], sum1);
    sum0 = fmaf(dp[2], wp[2], sum0);
    sum1 = fmaf(dp[3], wp[3], sum1);
    rb = sum0 + sum1;
  }
#pragma unroll
  for (int o = 32; o > 0; o >>= 1) {
    ra += __shfl_xor(ra, o, 64);
    rb += __shfl_xor(rb, o, 64);
  }
  if (lane == 0) {
    f32x2 r = {ra, rb};
    *(f32x2*)(ws + WS_LP + (size_t)(b0 + wave * 2) * 4) = r;
  }
}

constexpr int L_TMAIN = 0, L_TSIDE = 131072, L_WST = 147456;

__device__ __forceinline__ unsigned lds_rd32(unsigned addr) { return *(const LDS_AS unsigned*)addr; }
__device__ __forceinline__ bf16x8 lds_rd128(unsigned addr) { return *(const LDS_AS bf16x8*)addr; }

__device__ __forceinline__ float gelu_tanh(float x) {
  const float k0 = -2.f * 0.7978845608028654f * 1.4426950408889634f;
  const float k1 = k0 * 0.044715f;
  const float u = x * fmaf(x * x, k1, k0);
  return x * __builtin_amdgcn_rcpf(1.f + __builtin_amdgcn_exp2f(u));
}

__global__ __launch_bounds__(512, 2) void k2_mlp(const unsigned short* __restrict__ b1,
                                                 const unsigned short* __restrict__ w2,
                                                 char* __restrict__ ws) {
  __shared__ __attribute__((aligned(16))) char smem[163840];
  const int tid = threadIdx.x, lane = tid & 63;
  const int wave = __builtin_amdgcn_readfirstlane(tid >> 6);
  const int hp = blockIdx.x;
  const unsigned lbase = (unsigned)(uintptr_t)(LDS_AS char*)smem;

  const unsigned short* wt = (const unsigned short*)(ws + WS_WT);
  const int pc_half = wave >> 2, pc_c = (wave >> 1) & 1, pc_s = (wave & 1) * 2;
  const unsigned short* wsrc =
      wt + (size_t)(2 * hp + pc_c) * KD + (pc_half * 16 + (lane & 15)) * D_ + pc_s * 32 + (lane >> 4) * 8;
  u32x4 w0a = *(const u32x4*)(wsrc), w0b = *(const u32x4*)(wsrc + 32);
  u32x4 w1a = *(const u32x4*)(wsrc + 32 * D_), w1b = *(const u32x4*)(wsrc + 32 * D_ + 32);

  bf16x8 tab[8][4];
  {
    const char* tf = ws + WS_TAB + (size_t)wave * 32 * 1024 + lane * 16;
#pragma unroll
    for (int vt = 0; vt < 8; ++vt)
#pragma unroll
      for (int s = 0; s < 4; ++s) tab[vt][s] = *(const bf16x8*)(tf + (vt * 4 + s) * 1024);
  }
  u32x4 w2a = *(const u32x4*)(wsrc + 64 * D_), w2b = *(const u32x4*)(wsrc + 64 * D_ + 32);

  const unsigned wst_off = wave * 2048 + lane * 16;
  *(LDS_AS u32x4*)(lbase + L_WST + wst_off) = w0a;
  *(LDS_AS u32x4*)(lbase + L_WST + wst_off + 1024) = w0b;
  *(LDS_AS u32x4*)(lbase + L_TSIDE + wst_off) = w1a;
  *(LDS_AS u32x4*)(lbase + L_TSIDE + wst_off + 1024) = w1b;
  __syncthreads();

  const unsigned sel = (lane & 1) ? 0x3F800000u : 0x00003F80u;
  const unsigned s0 = (lane & 2) ? 0u : sel, s1 = (lane & 2) ? sel : 0u;
  const u32x2 a0u = {s0, s0}, a1u = {s1, s1};
  const bf16x4 selA0 = __builtin_bit_cast(bf16x4, a0u), selA1 = __builtin_bit_cast(bf16x4, a1u);

  f32x4 acc[4];
#pragma unroll
  for (int q = 0; q < 4; ++q) acc[q] = f32x4{0.f, 0.f, 0.f, 0.f};

  const unsigned t_wr = lbase + L_TMAIN + wave * 16384 + (lane >> 4) * 512 + (lane & 15) * 4;
  const char* idx_w = ws + WS_IDX + (size_t)wave * 32 * 1024 + lane * 16;

  constexpr int PF = 6;
#pragma unroll 1
  for (int p = 0; p < 3; ++p) {
    const unsigned wbase = lbase + (p == 1 ? L_TSIDE : L_WST) + lane * 16;
    const char* ip = idx_w + (size_t)p * 8 * 32 * 1024;
    u32x4 ix[32];
#pragma unroll
    for (int half = 0; half < 2; ++half) {
      bf16x8 wf[2][4];
#pragma unroll
      for (int c = 0; c < 2; ++c)
#pragma unroll
        for (int s = 0; s < 4; ++s) wf[c][s] = lds_rd128(wbase + ((half * 2 + c) * 4 + s) * 1024);
      if (half == 1) {
#pragma unroll
        for (int i = 0; i < PF; ++i) ix[i] = *(const u32x4*)(ip + i * 1024);
      }
#pragma unroll
      for (int vt = 0; vt < 8; ++vt) {
        f32x4 x = {0.f, 0.f, 0.f, 0.f}, y = {0.f, 0.f, 0.f, 0.f};
#pragma unroll
        for (int s = 0; s < 4; ++s) {
          x = __builtin_amdgcn_mfma_f32_16x16x32_bf16(tab[vt][s], wf[0][s], x, 0, 0, 0);
          y = __builtin_amdgcn_mfma_f32_16x16x32_bf16(tab[vt][s], wf[1][s], y, 0, 0, 0);
        }
#pragma unroll
        for (int r = 0; r < 4; ++r)
          *(LDS_AS unsigned*)(t_wr + vt * 2048 + r * 128 + half * 64) = pack_bf16(x[r], y[r]);
      }
    }
    if (p == 2) {
      const char* is = ws + WS_IDXS + ((size_t)wave * 512 + lane) * 8;
#pragma unroll
      for (int m = 0; m < 8; ++m) {
        const u32x2 x = *(const u32x2*)(is + m * 512);
        u32x2 d0 = {lds_rd32(lbase + (x[0] & 0xffffu) * 4), lds_rd32(lbase + (x[0] >> 16) * 4)};
        u32x2 d1 = {lds_rd32(lbase + (x[1] & 0xffffu) * 4), lds_rd32(lbase + (x[1] >> 16) * 4)};
        acc[m >> 1] = __builtin_amdgcn_mfma_f32_4x4x4bf16_1k((m & 1) ? selA1 : selA0, __builtin_bit_cast(bf16x4, d0),
                                                             acc[m >> 1], 0, 0, 0);
        acc[m >> 1] = __builtin_amdgcn_mfma_f32_4x4x4bf16_1k((m & 1) ? selA1 : selA0, __builtin_bit_cast(bf16x4, d1),
                                                             acc[m >> 1], 0, 0, 0);
      }
    }
    __syncthreads();
    if (p == 0) {
      *(LDS_AS u32x4*)(lbase + L_WST + wst_off) = w2a;
      *(LDS_AS u32x4*)(lbase + L_WST + wst_off + 1024) = w2b;
    }
    if (p == 1) {
      bf16x8 wsd[4];
      const unsigned short* src = wt + (size_t)(2 * hp + (lane & 1)) * KD + (96 + ((lane & 7) >> 1)) * D_ + (lane >> 4) * 8;
#pragma unroll
      for (int s = 0; s < 4; ++s) wsd[s] = *(const bf16x8*)(src + s * 32);
      const unsigned ts_wr = lbase + L_TSIDE + wave * 2048 + (lane >> 4) * 64 + (lane & 7) * 2;
#pragma unroll
      for (int vt = 0; vt < 8; ++vt) {
        f32x4 x = {0.f, 0.f, 0.f, 0.f};
#pragma unroll
        for (int s = 0; s < 4; ++s) x = __builtin_amdgcn_mfma_f32_16x16x32_bf16(tab[vt][s], wsd[s], x, 0, 0, 0);
#pragma unroll
        for (int r = 0; r < 4; ++r)
          *(LDS_AS unsigned short*)(ts_wr + vt * 256 + r * 16) = (unsigned short)pack_bf16(x[r], 0.f);
      }
    }
    constexpr int LAG = 5;
    u32x2 d[128];
#pragma unroll
    for (int k = 0; k < 128 + LAG; ++k) {
      if (k < 128) {
        const int it = k >> 2;
        if ((k & 3) == 0 && it + PF < 32) ix[it + PF] = *(const u32x4*)(ip + (it + PF) * 1024);
        const unsigned x = ix[it][k & 3];
        d[k][0] = lds_rd32(lbase + (x & 0xffffu) * 4);
        d[k][1] = lds_rd32(lbase + (x >> 16) * 4);
      }
      if (k >= LAG) {
        const int m = (k - LAG) >> 4;
        acc[m >> 1] = __builtin_amdgcn_mfma_f32_4x4x4bf16_1k((m & 1) ? selA1 : selA0,
                                                             __builtin_bit_cast(bf16x4, d[k - LAG]), acc[m >> 1], 0, 0, 0);
      }
      __builtin_amdgcn_sched_barrier(0);
    }
    if (p < 2) __syncthreads();
  }

  const unsigned b1u = *(const unsigned*)(b1 + 2 * hp), w2u = *(const unsigned*)(w2 + 2 * hp);
  const float b10 = __uint_as_float(b1u << 16), b11 = __uint_as_float(b1u & 0xffff0000u);
  const float w20 = __uint_as_float(w2u << 16), w21 = __uint_as_float(w2u & 0xffff0000u);
  float* part = (float*)(ws + WS_PART) + ((size_t)(wave * 16 + (lane >> 5)) * 256 + hp) * 32 + (lane & 31);
#pragma unroll
  for (int m = 0; m < 8; ++m) {
    const float z0 = acc[m >> 1][(m & 1) * 2] + b10, z1 = acc[m >> 1][(m & 1) * 2 + 1] + b11;
    part[(size_t)m * 2 * 256 * 32] = gelu_tanh(z0) * w20 + gelu_tanh(z1) * w21;
  }
}

__global__ __launch_bounds__(512) void k3_reduce(const unsigned short* __restrict__ b2, const char* __restrict__ ws,
                                                 unsigned short* __restrict__ out) {
  __shared__ float red[8][32];
  const int tid = threadIdx.x, lane = tid & 63, wave = tid >> 6;
  const f32x4* p = (const f32x4*)(ws + WS_PART + (size_t)blockIdx.x * 32768) + tid;
  f32x4 a = p[0], b = p[512], c = p[1024], d = p[1536];
  float lp = 0.f;
  if (tid < 32) lp = *(const float*)(ws + WS_LP + (size_t)(blockIdx.x * 32 + tid) * 4);
  f32x4 s = (a + b) + (c + d);
#pragma unroll
  for (int o = 8; o < 64; o <<= 1) {
#pragma unroll
    for (int r = 0; r < 4; ++r) s[r] += __shfl_xor(s[r], o, 64);
  }
  if (lane < 8) *(f32x4*)&red[wave][lane * 4] = s;
  __syncthreads();
  if (tid < 32) {
    float t = 0.f;
#pragma unroll
    for (int w = 0; w < 8; ++w) t += red[w][tid];
    const float ld = round_bf16(round_bf16(t) + bf2f(b2[0]));
    out[blockIdx.x * 32 + tid] = (unsigned short)pack_bf16(round_bf16(lp) + ld, 0.f);
  }
}

extern "C" void kernel_launch(void* const* d_in, const int* in_sizes, int n_in, void* d_out, int out_size, void* d_ws,
                              size_t ws_size, hipStream_t stream) {
  const int* idx = (const int*)d_in[0];
  const unsigned short* table = (const unsigned short*)d_in[1];
  const unsigned short* w_p = (const unsigned short*)d_in[2];
  const unsigned short* w1 = (const unsigned short*)d_in[3];
  const unsigned short* b1 = (const unsigned short*)d_in[4];
  const unsigned short* w2 = (const unsigned short*)d_in[5];
  const unsigned short* b2 = (const unsigned short*)d_in[6];
  char* ws = (char*)d_ws;
  k1_prep_pnn<<<256, 512, 0, stream>>>(idx, table, w_p, w1, ws);
  k2_mlp<<<256, 512, 0, stream>>>(b1, w2, ws);
  k3_reduce<<<128, 512, 0, stream>>>(b2, ws, (unsigned short*)d_out);
}
```

```cpp
#include <hip/hip_runtime.h>
#include <stdint.h>

constexpr size_t WS_REG0 = 1 << 20, WS_REGSZ = 18 << 20;

__global__ __launch_bounds__(512, 2) void fused(const int* __restrict__ idx, const unsigned short* __restrict__ table,
                                                const unsigned short* __restrict__ w_p, const unsigned short* __restrict__ w1,
                                                const unsigned short* __restrict__ b1, const unsigned short* __restrict__ w2,
                                                const unsigned short* __restrict__ b2, char* __restrict__ ws,
                                                unsigned short* __restrict__ out, unsigned long long rot_mask) {
  __shared__ __attribute__((aligned(16))) char smem[163840];
  unsigned tid = threadIdx.x;
  unsigned lds0 = (unsigned)(uintptr_t)(__attribute__((address_space(3))) char*)smem;
  asm volatile(
    "L_START_%=:\n"
    "s_getpc_b64 s[12:13]\n"
    "s_load_dwordx16 s[76:91], %[ka], 0x0\n"
    "s_load_dwordx4 s[28:31], %[ka], 0x40\n"
    "s_setprio 3\n"
    "v_lshlrev_b32 v202, 7, v0\n"
    "v_lshrrev_b32 v203, 6, v0\n"
    "s_lshr_b32 s14, %[wg], 3\n"
    "s_mov_b32 s15, 0x20000\n"
    "v_readfirstlane_b32 s10, v203\n"
    "v_and_b32 v136, 63, v0\n"
    "s_nop 0\n"
    "s_xor_b32 s14, s14, s10\n"
    "s_and_b32 s14, s14, 7\n"
    "s_cselect_b32 s14, 0, L_ENDK_%=-L_START_%=\n"
    "buffer_load_dword v2, v202, s[12:15], 0 offen\n"
    "v_lshlrev_b32 v57, 4, v136\n"
    "s_cmp_eq_u32 s10, 7\n"
    "s_cbranch_scc1 L_q7_%=\n"
    "s_cmp_lt_u32 s10, 4\n"
    "s_cbranch_scc0 L_qb_%=\n"
    "L_qa_%=:\n"
    "s_and_b32 s63, %[wg], 1\n"
    "s_lshr_b32 s65, %[wg], 1\n"
    "s_lshl2_add_u32 s65, s65, s63\n"
    "s_lshl3_add_u32 s65, s65, s10\n"
    "s_mul_i32 s61, s65, 400\n"
    "s_mul_i32 s60, s10, 800\n"
    "s_add_u32 s60, s60, 131072\n"
    "v_cmp_gt_u32_e64 vcc, 25, v136\n"
    "v_add_u32 v209, 6000, v57\n"
    "v_lshrrev_b32 v137, 4, v136\n"
    "v_cndmask_b32_e64 v54, v209, v57, vcc\n"
    "s_bfe_u32 s58, %[wg], 0x30003\n"
    "s_lshr_b32 s64, %[wg], 6\n"
    "s_bfe_u32 s62, %[wg], 0x20001\n"
    "s_lshl4_add_u32 s65, s62, s63\n"
    "s_lshl1_add_u32 s65, s58, s65\n"
    "s_mul_i32 s66, s64, 96\n"
    "v_and_b32 v206, 15, v136\n"
    "s_lshl2_add_u32 s65, s65, s10\n"
    "s_mul_i32 s67, s65, 6400\n"
    "s_add_u32 s67, s67, s66\n"
    "s_movk_i32 s66, 400\n"
    "v_and_b32 v204, 48, v136\n"
    "v_mad_u32_u24 v204, v206, s66, v204\n"
    "s_and_b32 s33, %[wg], 8\n"
    "s_lshl_b32 s55, s10, 14\n"
    "s_lshl_b32 s33, s33, 4\n"
    "s_lshl1_add_u32 s56, s55, s33\n"
    "v_bitop3_b32 v209, v136, v137, 2 bitop3:0x78\n"
    "v_lshrrev_b32 v208, 3, v136\n"
    "v_lshlrev_b32 v209, 4, v209\n"
    "v_lshl_add_u32 v48, v208, 7, v209\n"
    "v_xor_b32 v49, 0x840, v48\n"
    "s_mov_b32 s6, 1638400\n"
    "s_mov_b32 s7, s15\n"
    "s_mov_b32 s38, 256000\n"
    "s_mov_b32 s39, s15\n"
    "s_mov_b32 s42, 20000\n"
    "s_mov_b32 s43, s15\n"
    "s_mov_b32 s46, 13107200\n"
    "s_mov_b32 s47, s15\n"
    "s_mov_b32 s50, 18874368\n"
    "s_mov_b32 s51, s15\n"
    "s_waitcnt lgkmcnt(0)\n"
    "s_mov_b64 s[4:5], s[76:77]\n"
    "s_mov_b64 s[36:37], s[78:79]\n"
    "s_mov_b64 s[40:41], s[80:81]\n"
    "s_mov_b64 s[26:27], s[90:91]\n"
    "s_mov_b64 s[44:45], s[82:83]\n"
    "s_mov_b64 s[20:21], s[84:85]\n"
    "s_mov_b64 s[22:23], s[86:87]\n"
    "s_mov_b64 s[24:25], s[88:89]\n"
    "buffer_load_dwordx4 v[220:223], v204, s[4:7], s67 offen\n"
    "s_load_dword s12, s[26:27], 0x0\n"
    "s_mov_b32 m0, s60\n"
    "s_mov_b32 exec_hi, 0x3ffff\n"
    "buffer_load_dwordx4 v54, s[4:7], s61 offen lds\n"
    "s_mov_b32 exec_hi, -1\n"
    "s_setprio 2\n"
    "s_add_u32 m0, s55, 0\n"
    "s_add_u32 s16, s56, 0\n"
    "buffer_load_dwordx4 v48, s[36:39], s16 offen lds\n"
    "s_add_u32 m0, s55, 1024\n"
    "s_nop 0\n"
    "buffer_load_dwordx4 v49, s[36:39], s16 offen lds\n"
    "s_and_b32 s52, %[wg], 4\n"
    "s_add_u32 s52, s52, s64\n"
    "s_lshl3_add_u32 s52, s52, s58\n"
    "s_lshl_b32 s52, s52, 4\n"
    "s_load_dword s98, s[24:25], 0x0\n"
    "s_load_dwordx2 s[100:101], s[22:23], s52 offset:8\n"
    "s_load_dwordx2 s[24:25], s[22:23], s52\n"
    "s_load_dwordx4 s[20:23], s[20:21], s52\n"
    "s_load_dword s13, s[28:29], 0x0\n"
    "s_lshr_b32 s66, %[wg], 3\n"
    "s_and_b32 s53, %[wg], 3\n"
    "s_mul_i32 s53, s53, 6\n"
    "s_add_u32 s53, s53, s10\n"
    "s_lshl_b32 s53, s53, 5\n"
    "s_add_u32 s53, s53, s66\n"
    "s_lshl_b32 s53, s53, 14\n"
    "s_load_dword s13, s[44:45], s53\n"
    "L_ra_%=:\n"
    "v_and_b32 v138, 3, v136\n"
    "v_and_b32 v168, 1, v137\n"
    "v_lshrrev_b32 v216, 2, v206\n"
    "v_lshlrev_b32 v200, 3, v138\n"
    "v_cmp_eq_u32_e64 vcc, 0, v138\n"
    "v_add_u32 v217, 4, v206\n"
    "v_lshrrev_b32 v217, 2, v217\n"
    "v_cndmask_b32_e64 v201, 0, v216, vcc\n"
    "v_and_b32 v217, 2, v217\n"
    "v_lshl_add_u32 v249, v206, 2, s60\n"
    "v_lshl_add_u32 v250, v216, 2, s60\n"
    "v_lshlrev_b32 v139, 4, v137\n"
    "s_movk_i32 s31, 96\n"
    "s_lshl_b32 s65, s10, 14\n"
    "v_lshl_or_b32 v209, v138, 2, v216\n"
    "v_lshlrev_b32 v208, 3, v209\n"
    "v_bitop3_b32 v208, v208, v139, s31 bitop3:0x6c\n"
    "v_lshl_or_b32 v50, v209, 7, v208\n"
    "v_add_u32 v50, s65, v50\n"
    "v_xor_b32 v51, 64, v50\n"
    "v_add_u32 v52, 139392, v57\n"
    "v_lshl_or_b32 v53, v216, 2, v137\n"
    "v_lshl_or_b32 v53, v53, 2, v138\n"
    "v_lshlrev_b32 v53, 2, v53\n"
    "v_add_u32 v53, 139392, v53\n"
    "v_lshlrev_b32 v170, 4, v206\n"
    "s_lshl2_add_u32 s65, s10, 128000\n"
    "v_lshl_add_u32 v170, v137, 9, v170\n"
    "v_mul_u32_u24 v171, 0x100010, v168\n"
    "v_add_u32 v170, s65, v170\n"
    "v_add_u32 v172, 0x300020, v171\n"
    "v_add_u32 v171, 0x100000, v171\n"
    "s_add_u32 m0, s55, 2048\n"
    "s_add_u32 s16, s56, 4096\n"
    "buffer_load_dwordx4 v48, s[36:39], s16 offen lds\n"
    "s_add_u32 m0, s55, 3072\n"
    "s_nop 0\n"
    "buffer_load_dwordx4 v49, s[36:39], s16 offen lds\n"
    "s_waitcnt lgkmcnt(0)\n"
    "s_and_b32 s65, s12, s30\n"
    "s_add_u32 s12, s12, 1\n"
    "s_mul_i32 s13, s12, 0x9e3779b1\n"
    "s_add_u32 s13, s13, 0x7f4a7c15\n"
    "s_mul_i32 s65, s65, 18874368\n"
    "s_add_u32 s65, s65, 1048576\n"
    "s_add_u32 s48, s26, s65\n"
    "s_addc_u32 s49, s27, 0\n"
    "s_waitcnt vmcnt(5)\n"
    "s_setprio 3\n"
    "v_lshlrev_b32 v188, 3, v220\n"
    "v_lshlrev_b32 v189, 11, v221\n"
    "v_and_b32 v188, 0x18, v188\n"
    "v_and_b32 v189, 0x1800, v189\n"
    "v_lshlrev_b32 v190, 19, v222\n"
    "v_lshlrev_b32 v191, 27, v223\n"
    "v_and_b32 v190, 0x180000, v190\n"
    "v_and_b32 v191, 0x18000000, v191\n"
    "v_or3_b32 v202, v188, v189, v190\n"
    "v_or_b32 v202, v202, v191\n"
    "s_nop 1\n"
    "v_mov_b32_dpp v188, v202 quad_perm:[0,0,0,0] row_mask:0xf bank_mask:0xf\n"
    "v_mov_b32_dpp v189, v202 quad_perm:[1,1,1,1] row_mask:0xf bank_mask:0xf\n"
    "v_mov_b32_dpp v232, v202 row_newbcast:2 row_mask:0xf bank_mask:0xf\n"
    "v_bfe_u32 v232, v232, v200, 5\n"
    "v_lshlrev_b32_e64 v184, v232, 3\n"
    "v_bfe_u32 v188, v188, v200, 5\n"
    "v_bfe_u32 v189, v189, v200, 5\n"
    "v_lshlrev_b32_e64 v188, v188, 3\n"
    "v_lshl_add_u32 v203, 3, v189, v188\n"
    "v_bfe_u32 v248, v203, v232, 5\n"
    "v_lshl_add_u32 v248, 4, v248, v201\n"
    "v_mov_b32_dpp v233, v202 row_newbcast:6 row_mask:0xf bank_mask:0xf\n"
    "v_bfe_u32 v233, v233, v200, 5\n"
    "v_add_u32_dpp v248, v248, v248 quad_perm:[1,0,3,2] row_mask:0xf bank_mask:0xf\n"
    "v_lshlrev_b32_e64 v185, v233, 3\n"
    "v_lshl_add_u32 v253, v220, 6, v171\n"
    "v_add_u32_dpp v248, v248, v248 quad_perm:[2,3,0,1] row_mask:0xf bank_mask:0xf\n"
    "s_waitcnt vmcnt(4)\n"
    "ds_read_b32 v0, v250 offset:384\n"
    "v_min_u32_dpp v251, v248, v248 row_ror:4 row_mask:0xf bank_mask:0xf\n"
    "ds_read_b32 v8, v250 offset:784\n"
    "ds_read_b32 v16, v249\n"
    "v_min_u32_dpp v252, v251, v251 row_ror:8 row_mask:0xf bank_mask:0xf\n"
    "v_cmp_eq_u32_e64 vcc, v252, v248\n"
    "ds_read_b32 v24, v249 offset:400\n"
    "s_nop 0\n"
    "v_cndmask_b32_e64 v184, 0, v184, vcc\n"
    "v_mov_b32_dpp v218, v252 quad_perm:[0,1,2,3] row_mask:0xf bank_mask:0x1\n"
    "v_add_u32 v203, v203, v184\n"
    "v_bfe_u32 v248, v203, v233, 5\n"
    "v_lshl_add_u32 v248, 4, v248, v201\n"
    "v_mov_b32_dpp v234, v202 row_newbcast:10 row_mask:0xf bank_mask:0xf\n"
    "v_bfe_u32 v234, v234, v200, 5\n"
    "v_add_u32_dpp v248, v248, v248 quad_perm:[1,0,3,2] row_mask:0xf bank_mask:0xf\n"
    "v_lshlrev_b32_e64 v186, v234, 3\n"
    "v_lshl_add_u32 v253, v221, 22, v253\n"
    "v_add_u32_dpp v248, v248, v248 quad_perm:[2,3,0,1] row_mask:0xf bank_mask:0xf\n"
    "ds_read_b32 v32, v249 offset:64\n"
    "ds_read_b32 v40, v249 offset:464\n"
    "v_min_u32_dpp v251, v248, v248 row_ror:4 row_mask:0xf bank_mask:0xf\n"
    "ds_read_b32 v64, v249 offset:128\n"
    "ds_read_b32 v72, v249 offset:528\n"
    "v_min_u32_dpp v252, v251, v251 row_ror:8 row_mask:0xf bank_mask:0xf\n"
    "v_cmp_eq_u32_e64 vcc, v252, v248\n"
    "ds_read_b32 v80, v249 offset:192\n"
    "s_nop 0\n"
    "v_cndmask_b32_e64 v185, 0, v185, vcc\n"
    "v_mov_b32_dpp v218, v252 quad_perm:[0,1,2,3] row_mask:0xf bank_mask:0x2\n"
    "v_add_u32 v203, v203, v185\n"
    "v_bfe_u32 v248, v203, v234, 5\n"
    "v_lshl_add_u32 v248, 4, v248, v201\n"
    "v_mov_b32_dpp v235, v202 row_newbcast:14 row_mask:0xf bank_mask:0xf\n"
    "v_bfe_u32 v235, v235, v200, 5\n"
    "v_add_u32_dpp v248, v248, v248 quad_perm:[1,0,3,2] row_mask:0xf bank_mask:0xf\n"
    "v_lshlrev_b32_e64 v187, v235, 3\n"
    "v_lshl_add_u32 v254, v222, 6, v172\n"
    "v_add_u32_dpp v248, v248, v248 quad_perm:[2,3,0,1] row_mask:0xf bank_mask:0xf\n"
    "ds_read_b32 v88, v249 offset:592\n"
    "ds_read_b32 v96, v249 offset:256\n"
    "v_min_u32_dpp v251, v248, v248 row_ror:4 row_mask:0xf bank_mask:0xf\n"
    "ds_read_b32 v104, v249 offset:656\n"
    "ds_read_b32 v112, v249 offset:320\n"
    "v_min_u32_dpp v252, v251, v251 row_ror:8 row_mask:0xf bank_mask:0xf\n"
    "v_cmp_eq_u32_e64 vcc, v252, v248\n"
    "ds_read_b32 v120, v249 offset:720\n"
    "s_nop 0\n"
    "v_cndmask_b32_e64 v186, 0, v186, vcc\n"
    "v_mov_b32_dpp v218, v252 quad_perm:[0,1,2,3] row_mask:0xf bank_mask:0x4\n"
    "v_add_u32 v203, v203, v186\n"
    "v_bfe_u32 v248, v203, v235, 5\n"
    "v_lshl_add_u32 v248, 4, v248, v201\n"
    "v_mov_b32_dpp v236, v202 row_newbcast:3 row_mask:0xf bank_mask:0xf\n"
    "v_bfe_u32 v236, v236, v200, 5\n"
    "v_add_u32_dpp v248, v248, v248 quad_perm:[1,0,3,2] row_mask:0xf bank_mask:0xf\n"
    "v_lshlrev_b32_e64 v188, v236, 3\n"
    "v_lshl_add_u32 v254, v223, 22, v254\n"
    "v_add_u32_dpp v248, v248, v248 quad_perm:[2,3,0,1] row_mask:0xf bank_mask:0xf\n"
    "s_waitcnt lgkmcnt(12)\n"
    "s_mov_b32 exec_lo, 0xeeeeeeee\n"
    "s_mov_b32 exec_hi, 0xeeeeeeee\n"
    "v_mov_b32 v0, 1275\n"
    "v_mov_b32 v8, 1275\n"
    "s_mov_b64 exec, -1\n"
    "v_min_u32_dpp v251, v248, v248 row_ror:4 row_mask:0xf bank_mask:0xf\n"
    "v_lshlrev_b32 v4, 3, v0\n"
    "v_bitop3_b32 v4, v4, v139, s31 bitop3:0x6c\n"
    "v_min_u32_dpp v252, v251, v251 row_ror:8 row_mask:0xf bank_mask:0xf\n"
    "v_cmp_eq_u32_e64 vcc, v252, v248\n"
    "v_lshl_or_b32 v0, v0, 7, v4\n"
    "v_xor_b32 v4, 64, v0\n"
    "v_cndmask_b32_e64 v187, 0, v187, vcc\n"
    "v_mov_b32_dpp v218, v252 quad_perm:[0,1,2,3] row_mask:0xf bank_mask:0x8\n"
    "v_add_u32 v203, v203, v187\n"
    "v_bfe_u32 v248, v203, v236, 5\n"
    "v_lshl_add_u32 v248, 4, v248, v201\n"
    "v_mov_b32_dpp v237, v202 row_newbcast:7 row_mask:0xf bank_mask:0xf\n"
    "v_bfe_u32 v237, v237, v200, 5\n"
    "v_add_u32_dpp v248, v248, v248 quad_perm:[1,0,3,2] row_mask:0xf bank_mask:0xf\n"
    "v_lshlrev_b32_e64 v189, v237, 3\n"
    "v_lshlrev_b32 v12, 3, v8\n"
    "v_add_u32_dpp v248, v248, v248 quad_perm:[2,3,0,1] row_mask:0xf bank_mask:0xf\n"
    "v_bitop3_b32 v12, v12, v139, s31 bitop3:0x6c\n"
    "v_lshl_or_b32 v8, v8, 7, v12\n"
    "v_min_u32_dpp v251, v248, v248 row_ror:4 row_mask:0xf bank_mask:0xf\n"
    "v_xor_b32 v12, 64, v8\n"
    "s_waitcnt lgkmcnt(11)\n"
    "v_lshlrev_b32 v20, 3, v16\n"
    "v_min_u32_dpp v252, v251, v251 row_ror:8 row_mask:0xf bank_mask:0xf\n"
    "v_cmp_eq_u32_e64 vcc, v252, v248\n"
    "s_nop 1\n"
    "v_cndmask_b32_e64 v188, 0, v188, vcc\n"
    "v_mov_b32_dpp v219, v252 quad_perm:[0,1,2,3] row_mask:0xf bank_mask:0x1\n"
    "v_add_u32 v203, v203, v188\n"
    "v_bfe_u32 v248, v203, v237, 5\n"
    "v_lshl_add_u32 v248, 4, v248, v201\n"
    "v_mov_b32_dpp v238, v202 row_newbcast:11 row_mask:0xf bank_mask:0xf\n"
    "v_bfe_u32 v238, v238, v200, 5\n"
    "v_add_u32_dpp v248, v248, v248 quad_perm:[1,0,3,2] row_mask:0xf bank_mask:0xf\n"
    "v_lshlrev_b32_e64 v190, v238, 3\n"
    "v_bitop3_b32 v20, v20, v139, s31 bitop3:0x6c\n"
    "v_add_u32_dpp v248, v248, v248 quad_perm:[2,3,0,1] row_mask:0xf bank_mask:0xf\n"
    "v_lshl_or_b32 v16, v16, 7, v20\n"
    "v_xor_b32 v20, 64, v16\n"
    "v_min_u32_dpp v251, v248, v248 row_ror:4 row_mask:0xf bank_mask:0xf\n"
    "s_waitcnt lgkmcnt(10)\n"
    "v_lshlrev_b32 v28, 3, v24\n"
    "v_min_u32_dpp v252, v251, v251 row_ror:8 row_mask:0xf bank_mask:0xf\n"
    "v_cmp_eq_u32_e64 vcc, v252, v248\n"
    "v_bitop3_b32 v28, v28, v139, s31 bitop3:0x6c\n"
    "s_nop 0\n"
    "v_cndmask_b32_e64 v189, 0, v189, vcc\n"
    "v_mov_b32_dpp v219, v252 quad_perm:[0,1,2,3] row_mask:0xf bank_mask:0x2\n"
    "v_add_u32 v203, v203, v189\n"
    "v_bfe_u32 v248, v203, v238, 5\n"
    "v_lshl_add_u32 v248, 4, v248, v201\n"
    "v_mov_b32_dpp v239, v202 row_newbcast:15 row_mask:0xf bank_mask:0xf\n"
    "v_bfe_u32 v239, v239, v200, 5\n"
    "v_add_u32_dpp v248, v248, v248 quad_perm:[1,0,3,2] row_mask:0xf bank_mask:0xf\n"
    "v_lshlrev_b32_e64 v191, v239, 3\n"
    "v_lshl_or_b32 v24, v24, 7, v28\n"
    "v_add_u32_dpp v248, v248, v248 quad_perm:[2,3,0,1] row_mask:0xf bank_mask:0xf\n"
    "v_xor_b32 v28, 64, v24\n"
    "s_waitcnt lgkmcnt(9)\n"
    "v_lshlrev_b32 v36, 3, v32\n"
    "v_min_u32_dpp v251, v248, v248 row_ror:4 row_mask:0xf bank_mask:0xf\n"
    "v_bitop3_b32 v36, v36, v139, s31 bitop3:0x6c\n"
    "v_lshl_or_b32 v32, v32, 7, v36\n"
    "v_min_u32_dpp v252, v251, v251 row_ror:8 row_mask:0xf bank_mask:0xf\n"
    "v_cmp_eq_u32_e64 vcc, v252, v248\n"
    "s_nop 1\n"
    "v_cndmask_b32_e64 v190, 0, v190, vcc\n"
    "v_mov_b32_dpp v219, v252 quad_perm:[0,1,2,3] row_mask:0xf bank_mask:0x4\n"
    "v_add_u32 v203, v203, v190\n"
    "v_bfe_u32 v248, v203, v239, 5\n"
    "v_lshl_add_u32 v248, 4, v248, v201\n"
    "v_xor_b32 v36, 64, v32\n"
    "s_waitcnt lgkmcnt(8)\n"
    "v_lshlrev_b32 v44, 3, v40\n"
    "v_add_u32_dpp v248, v248, v248 quad_perm:[1,0,3,2] row_mask:0xf bank_mask:0xf\n"
    "v_bitop3_b32 v44, v44, v139, s31 bitop3:0x6c\n"
    "v_lshl_or_b32 v40, v40, 7, v44\n"
    "v_add_u32_dpp v248, v248, v248 quad_perm:[2,3,0,1] row_mask:0xf bank_mask:0xf\n"
    "v_xor_b32 v44, 64, v40\n"
    "s_nop 0\n"
    "v_min_u32_dpp v251, v248, v248 row_ror:4 row_mask:0xf bank_mask:0xf\n"
    "s_nop 1\n"
    "v_min_u32_dpp v252, v251, v251 row_ror:8 row_mask:0xf bank_mask:0xf\n"
    "s_nop 1\n"
    "v_mov_b32_dpp v219, v252 quad_perm:[0,1,2,3] row_mask:0xf bank_mask:0x8\n"
    "v_cmp_eq_u32_e64 vcc, 16, v200\n"
    "v_cmp_eq_u32_e64 s[14:15], 24, v200\n"
    "s_nop 0\n"
    "v_cndmask_b32_e64 v216, v216, v218, vcc\n"
    "v_cndmask_b32_e64 v216, v216, v219, s[14:15]\n"
    "v_lshlrev_b32 v188, 4, v216\n"
    "v_xor_b32 v189, v216, v217\n"
    "v_and_b32 v189, 2, v189\n"
    "v_cmp_eq_u32_e64 vcc, 0, v189\n"
    "v_alignbit_b32 v253, v253, v253, v188\n"
    "v_alignbit_b32 v254, v254, v254, v188\n"
    "v_cndmask_b32_e64 v188, v254, v253, vcc\n"
    "v_cndmask_b32_e64 v189, v253, v254, vcc\n"
    "ds_write2_b32 v170, v188, v189 offset1:64\n"
    "s_setprio 1\n"
    "s_waitcnt lgkmcnt(8)\n"
    "v_lshlrev_b32 v68, 3, v64\n"
    "v_bitop3_b32 v68, v68, v139, s31 bitop3:0x6c\n"
    "v_lshl_or_b32 v64, v64, 7, v68\n"
    "v_xor_b32 v68, 64, v64\n"
    "s_waitcnt lgkmcnt(7)\n"
    "v_lshlrev_b32 v76, 3, v72\n"
    "v_bitop3_b32 v76, v76, v139, s31 bitop3:0x6c\n"
    "v_lshl_or_b32 v72, v72, 7, v76\n"
    "v_xor_b32 v76, 64, v72\n"
    "s_waitcnt lgkmcnt(6)\n"
    "v_lshlrev_b32 v84, 3, v80\n"
    "v_bitop3_b32 v84, v84, v139, s31 bitop3:0x6c\n"
    "v_lshl_or_b32 v80, v80, 7, v84\n"
    "v_xor_b32 v84, 64, v80\n"
    "s_waitcnt lgkmcnt(5)\n"
    "v_lshlrev_b32 v92, 3, v88\n"
    "v_bitop3_b32 v92, v92, v139, s31 bitop3:0x6c\n"
    "v_lshl_or_b32 v88, v88, 7, v92\n"
    "v_xor_b32 v92, 64, v88\n"
    "s_waitcnt lgkmcnt(4)\n"
    "v_lshlrev_b32 v100, 3, v96\n"
    "v_bitop3_b32 v100, v100, v139, s31 bitop3:0x6c\n"
    "v_lshl_or_b32 v96, v96, 7, v100\n"
    "v_xor_b32 v100, 64, v96\n"
    "s_waitcnt lgkmcnt(3)\n"
    "v_lshlrev_b32 v108, 3, v104\n"
    "v_bitop3_b32 v108, v108, v139, s31 bitop3:0x6c\n"
    "v_lshl_or_b32 v104, v104, 7, v108\n"
    "v_xor_b32 v108, 64, v104\n"
    "s_waitcnt lgkmcnt(2)\n"
    "v_lshlrev_b32 v116, 3, v112\n"
    "v_bitop3_b32 v116, v116, v139, s31 bitop3:0x6c\n"
    "v_lshl_or_b32 v112, v112, 7, v116\n"
    "v_xor_b32 v116, 64, v112\n"
    "s_waitcnt lgkmcnt(1)\n"
    "v_lshlrev_b32 v124, 3, v120\n"
    "v_bitop3_b32 v124, v124, v139, s31 bitop3:0x6c\n"
    "v_lshl_or_b32 v120, v120, 7, v124\n"
    "v_xor_b32 v124, 64, v120\n"
    "s_and_b32 s11, %[wg], 3\n"
    "s_mov_b32 s65, s52\n"
    "s_lshl_b32 s52, s11, 10\n"
    "s_add_u32 s52, s52, s65\n"
    "v_lshrrev_b32 v141, 2, v136\n"
    "v_lshl_add_u32 v58, v136, 10, s65\n"
    "s_add_u32 s59, s10, s58\n"
    "s_and_b32 s59, s59, 7\n"
    "s_lshl_b32 s53, s59, 10\n"
    "s_and_b32 s65, s59, 2\n"
    "s_lshl_b32 s65, s65, 2\n"
    "s_add_u32 s53, s53, s65\n"
    "s_and_b32 s65, s59, 4\n"
    "s_lshl_b32 s65, s65, 4\n"
    "s_add_u32 s53, s53, s65\n"
    "s_add_u32 s53, s53, 131072\n"
    "s_lshl_b32 s59, s59, 7\n"
    "s_xor_b32 s59, s59, s33\n"
    "s_lshl_b32 s59, s59, 9\n"
    "s_mul_i32 s57, s11, 3145728\n"
    "s_lshl_b32 s54, s10, 13\n"
    "s_lshr_b32 s60, %[wg], 3\n"
    "s_lshl_b32 s60, s60, 7\n"
    "s_and_b32 s65, %[wg], 4\n"
    "s_add_u32 s60, s60, s65\n"
    "s_lshl_b32 s60, s60, 7\n"
    "v_lshlrev_b32 v56, 11, v138\n"
    "v_and_b32 v143, 1, v136\n"
    "v_lshl_or_b32 v56, v143, 3, v56\n"
    "v_and_b32 v140, 2, v136\n"
    "v_lshl_or_b32 v56, v140, 5, v56\n"
    "v_lshl_or_b32 v56, v137, 7, v56\n"
    "v_and_b32 v142, 3, v141\n"
    "v_lshl_or_b32 v56, v142, 4, v56\n"
    "v_add_u32 v56, 131072, v56\n"
    "v_lshrrev_b32 v55, 1, v57\n"
    "v_lshrrev_b32 v142, 1, v136\n"
    "v_xor_b32 v142, v142, v136\n"
    "v_and_or_b32 v142, v142, 8, v140\n"
    "v_mov_b32 v140, 0x3f80\n"
    "v_cmp_eq_u32_e64 vcc, 0, v142\n"
    "v_cmp_eq_u32_e64 s[14:15], 2, v142\n"
    "v_and_b32 v141, 1, v141\n"
    "v_lshl_or_b32 v141, v143, 1, v141\n"
    "v_mul_u32_u24 v59, 0x5555, v141\n"
    "v_cndmask_b32_e64 v60, 0, v140, vcc\n"
    "v_cndmask_b32_e64 v61, 0, v140, s[14:15]\n"
    "v_cndmask_b32_e64 v62, 0, v140, vcc\n"
    "v_cndmask_b32_e64 v63, 0, v140, s[14:15]\n"
    "s_add_u32 m0, s55, 4096\n"
    "s_add_u32 s16, s56, 8192\n"
    "buffer_load_dwordx4 v48, s[36:39], s16 offen lds\n"
    "s_add_u32 m0, s55, 5120\n"
    "s_nop 0\n"
    "buffer_load_dwordx4 v49, s[36:39], s16 offen lds\n"
    "s_add_u32 m0, s55, 6144\n"
    "s_add_u32 s16, s56, 12288\n"
    "buffer_load_dwordx4 v48, s[36:39], s16 offen lds\n"
    "s_add_u32 m0, s55, 7168\n"
    "s_nop 0\n"
    "buffer_load_dwordx4 v49, s[36:39], s16 offen lds\n"
    "s_add_u32 m0, s55, 8192\n"
    "s_add_u32 s16, s56, 16384\n"
    "buffer_load_dwordx4 v48, s[36:39], s16 offen lds\n"
    "s_add_u32 m0, s55, 9216\n"
    "s_nop 0\n"
    "buffer_load_dwordx4 v49, s[36:39], s16 offen lds\n"
    "s_add_u32 m0, s55, 10240\n"
    "s_add_u32 s16, s56, 20480\n"
    "buffer_load_dwordx4 v48, s[36:39], s16 offen lds\n"
    "s_add_u32 m0, s55, 11264\n"
    "s_nop 0\n"
    "buffer_load_dwordx4 v49, s[36:39], s16 offen lds\n"
    "s_add_u32 m0, s55, 12288\n"
    "s_add_u32 s16, s56, 24576\n"
    "buffer_load_dwordx4 v48, s[36:39], s16 offen lds\n"
    "s_add_u32 m0, s55, 13312\n"
    "s_nop 0\n"
    "buffer_load_dwordx4 v49, s[36:39], s16 offen lds\n"
    "s_add_u32 m0, s55, 14336\n"
    "s_add_u32 s16, s56, 28672\n"
    "buffer_load_dwordx4 v48, s[36:39], s16 offen lds\n"
    "s_add_u32 m0, s55, 15360\n"
    "s_nop 0\n"
    "buffer_load_dwordx4 v49, s[36:39], s16 offen lds\n"
    "s_waitcnt vmcnt(14)\n"
    "ds_read_b128 v[128:131], v50\n"
    "ds_read_b128 v[132:135], v51\n"
    "s_waitcnt vmcnt(12)\n"
    "ds_read_b128 v[144:147], v50 offset:2048\n"
    "ds_read_b128 v[148:151], v51 offset:2048\n"
    "s_waitcnt vmcnt(10)\n"
    "ds_read_b128 v[160:163], v50 offset:4096\n"
    "ds_read_b128 v[164:167], v51 offset:4096\n"
    "s_waitcnt vmcnt(8)\n"
    "ds_read_b128 v[176:179], v50 offset:6144\n"
    "ds_read_b128 v[180:183], v51 offset:6144\n"
    "s_waitcnt vmcnt(6)\n"
    "ds_read_b128 v[192:195], v50 offset:8192\n"
    "ds_read_b128 v[196:199], v51 offset:8192\n"
    "s_waitcnt vmcnt(4)\n"
    "ds_read_b128 v[208:211], v50 offset:10240\n"
    "ds_read_b128 v[212:215], v51 offset:10240\n"
    "s_waitcnt vmcnt(2)\n"
    "ds_read_b128 v[224:227], v50 offset:12288\n"
    "ds_read_b128 v[228:231], v51 offset:12288\n"
    "s_waitcnt vmcnt(0)\n"
    "ds_read_b128 v[240:243], v50 offset:14336\n"
    "ds_read_b128 v[244:247], v51 offset:14336\n"
    "s_and_b32 s65, %[wg], 4\n"
    "s_cbranch_scc1 L_n350_%=\n"
    "v_lshlrev_b32 v174, 6, v57\n"
    "s_and_b32 s65, %[wg], 3\n"
    "s_mul_i32 s65, s65, 3145728\n"
    "s_bfe_u32 s66, %[wg], 0x50003\n"
    "s_lshl2_add_u32 s66, s66, s10\n"
    "s_mul_i32 s66, s66, 12288\n"
    "s_add_u32 s65, s65, s66\n"
    "s_add_u32 s65, s65, 384\n"
    "s_mov_b32 m0, 163328\n"
    "s_bfm_b64 exec, 12, 0\n"
    "buffer_load_dword v174, s[44:47], s65 offen lds\n"
    "s_mov_b64 exec, -1\n"
    "L_n350_%=:\n"
    "s_sub_u32 s56, s56, s33\n"
    "s_xor_b32 s33, s33, 128\n"
    "s_add_u32 s56, s56, s33\n"
    "s_add_u32 s55, s54, 8188\n"
    "s_lshl_b32 s18, s52, 12\n"
    "s_add_u32 s18, s18, s54\n"
    "s_add_u32 s18, s18, 2097152\n"
    "s_add_u32 s19, s18, 4096\n"
    "s_mul_i32 s34, s11, 24576\n"
    "s_lshl_b32 s35, s10, 10\n"
    "s_add_u32 s34, s34, s35\n"
    "s_add_u32 s34, s34, 65536\n"
    "s_cmp_lt_u32 s10, 4\n"
    "s_cselect_b64 vcc, -1, 0\n"
    "v_subrev_u32 v50, s54, v50\n"
    "v_subrev_u32 v51, s54, v51\n"
    "s_waitcnt lgkmcnt(15)\n"
    "s_barrier\n"
    "s_branch L_RA_%=\n"
    "L_qb_%=:\n"
    "s_and_b32 s63, %[wg], 1\n"
    "s_lshr_b32 s65, %[wg], 1\n"
    "s_lshl2_add_u32 s65, s65, s63\n"
    "s_lshl3_add_u32 s65, s65, s10\n"
    "s_mul_i32 s61, s65, 400\n"
    "s_mul_i32 s60, s10, 800\n"
    "s_add_u32 s60, s60, 131072\n"
    "v_cmp_gt_u32_e64 vcc, 25, v136\n"
    "v_add_u32 v209, 6000, v57\n"
    "v_lshrrev_b32 v137, 4, v136\n"
    "v_cndmask_b32_e64 v54, v209, v57, vcc\n"
    "s_and_b32 s33, %[wg], 8\n"
    "s_lshl_b32 s55, s10, 14\n"
    "s_lshl_b32 s33, s33, 4\n"
    "s_lshl1_add_u32 s56, s55, s33\n"
    "v_bitop3_b32 v209, v136, v137, 2 bitop3:0x78\n"
    "v_lshrrev_b32 v208, 3, v136\n"
    "v_lshlrev_b32 v209, 4, v209\n"
    "v_lshl_add_u32 v48, v208, 7, v209\n"
    "v_xor_b32 v49, 0x840, v48\n"
    "v_lshlrev_b32 v205, 7, v136\n"
    "s_and_b32 s18, %[wg], 0xf8\n"
    "s_lshl_b32 s18, s18, 10\n"
    "s_mov_b32 s6, 1638400\n"
    "s_mov_b32 s7, s15\n"
    "s_mov_b32 s38, 256000\n"
    "s_mov_b32 s39, s15\n"
    "s_mov_b32 s42, 20000\n"
    "s_mov_b32 s43, s15\n"
    "s_mov_b32 s46, 13107200\n"
    "s_mov_b32 s47, s15\n"
    "s_mov_b32 s50, 18874368\n"
    "s_mov_b32 s51, s15\n"
    "s_waitcnt lgkmcnt(0)\n"
    "s_mov_b64 s[4:5], s[76:77]\n"
    "s_mov_b64 s[36:37], s[78:79]\n"
    "s_mov_b64 s[40:41], s[80:81]\n"
    "s_mov_b64 s[26:27], s[90:91]\n"
    "s_mov_b64 s[44:45], s[82:83]\n"
    "s_mov_b64 s[20:21], s[84:85]\n"
    "s_mov_b64 s[22:23], s[86:87]\n"
    "s_mov_b64 s[24:25], s[88:89]\n"
    "s_cmp_lg_u32 s10, 4\n"
    "s_cbranch_scc1 L_np_%=\n"
    "s_mov_b32 m0, 163328\n"
    "s_nop 0\n"
    "buffer_load_dword v205, s[36:39], s18 offen lds\n"
    "L_np_%=:\n"
    "s_mov_b32 m0, s60\n"
    "s_mov_b32 exec_hi, 0x3ffff\n"
    "buffer_load_dwordx4 v54, s[4:7], s61 offen lds\n"
    "s_mov_b32 exec_hi, -1\n"
    "s_load_dword s12, s[26:27], 0x0\n"
    "s_setprio 2\n"
    "s_add_u32 m0, s55, 0\n"
    "s_add_u32 s16, s56, 0\n"
    "buffer_load_dwordx4 v48, s[36:39], s16 offen lds\n"
    "s_add_u32 m0, s55, 1024\n"
    "s_nop 0\n"
    "buffer_load_dwordx4 v49, s[36:39], s16 offen lds\n"
    "s_add_u32 m0, s55, 2048\n"
    "s_add_u32 s16, s56, 4096\n"
    "buffer_load_dwordx4 v48, s[36:39], s16 offen lds\n"
    "s_add_u32 m0, s55, 3072\n"
    "s_nop 0\n"
    "buffer_load_dwordx4 v49, s[36:39], s16 offen lds\n"
    "L_rb_%=:\n"
    "s_bfe_u32 s58, %[wg], 0x30003\n"
    "s_lshr_b32 s64, %[wg], 6\n"
    "s_bfe_u32 s62, %[wg], 0x20001\n"
    "s_and_b32 s52, %[wg], 4\n"
    "s_add_u32 s52, s52, s64\n"
    "s_lshl3_add_u32 s52, s52, s58\n"
    "s_lshl_b32 s52, s52, 4\n"
    "s_load_dword s98, s[24:25], 0x0\n"
    "s_load_dwordx2 s[100:101], s[22:23], s52 offset:8\n"
    "s_load_dwordx2 s[24:25], s[22:23], s52\n"
    "s_load_dwordx4 s[20:23], s[20:21], s52\n"
    "s_load_dword s13, s[28:29], 0x0\n"
    "s_lshr_b32 s66, %[wg], 3\n"
    "s_and_b32 s53, %[wg], 3\n"
    "s_mul_i32 s53, s53, 6\n"
    "s_add_u32 s53, s53, s10\n"
    "s_cmp_lt_u32 s10, 6\n"
    "s_cselect_b32 s53, s53, 24\n"
    "s_lshl_b32 s53, s53, 5\n"
    "s_add_u32 s53, s53, s66\n"
    "s_lshl_b32 s53, s53, 14\n"
    "s_load_dword s13, s[44:45], s53\n"
    "s_add_u32 m0, s55, 4096\n"
    "s_add_u32 s16, s56, 8192\n"
    "buffer_load_dwordx4 v48, s[36:39], s16 offen lds\n"
    "s_lshl4_add_u32 s65, s62, s63\n"
    "s_lshl1_add_u32 s65, s58, s65\n"
    "s_mul_i32 s66, s64, 96\n"
    "v_and_b32 v206, 15, v136\n"
    "s_mul_i32 s67, s65, 25600\n"
    "s_mul_i32 s65, s10, 12800\n"
    "s_add_u32 s65, s65, s66\n"
    "s_add_u32 s65, s65, -51136\n"
    "s_cmp_eq_u32 s62, s64\n"
    "s_cselect_b32 s66, 384, 1073741824\n"
    "s_cmp_lt_u32 s10, 6\n"
    "s_cselect_b32 s65, s65, s66\n"
    "s_movk_i32 s35, 6400\n"
    "s_cselect_b32 s34, 16, s35\n"
    "s_cselect_b32 s35, s35, 12800\n"
    "s_add_u32 s67, s67, s65\n"
    "v_mul_u32_u24 v204, 400, v206\n"
    "v_and_b32 v168, 1, v137\n"
    "v_lshrrev_b32 v169, 5, v136\n"
    "v_mad_u32_u24 v204, v168, s34, v204\n"
    "v_mad_u32_u24 v204, v169, s35, v204\n"
    "v_and_b32 v138, 3, v136\n"
    "v_bfe_u32 v216, v136, 2, 2\n"
    "s_add_u32 m0, s55, 5120\n"
    "s_nop 0\n"
    "buffer_load_dwordx4 v49, s[36:39], s16 offen lds\n"
    "v_lshlrev_b32 v200, 3, v138\n"
    "v_cmp_eq_u32_e64 vcc, 0, v138\n"
    "v_add_u32 v217, 4, v206\n"
    "v_lshrrev_b32 v217, 2, v217\n"
    "v_cndmask_b32_e64 v201, 0, v216, vcc\n"
    "v_and_b32 v217, 2, v217\n"
    "s_add_u32 m0, s55, 6144\n"
    "s_add_u32 s16, s56, 12288\n"
    "buffer_load_dwordx4 v48, s[36:39], s16 offen lds\n"
    "v_lshl_add_u32 v249, v206, 2, s60\n"
    "v_lshl_add_u32 v250, v216, 2, s60\n"
    "v_lshlrev_b32 v139, 4, v137\n"
    "s_movk_i32 s31, 96\n"
    "s_lshl_b32 s65, s10, 14\n"
    "v_lshl_or_b32 v209, v138, 2, v216\n"
    "v_lshlrev_b32 v208, 3, v209\n"
    "v_bitop3_b32 v208, v208, v139, s31 bitop3:0x6c\n"
    "v_lshl_or_b32 v50, v209, 7, v208\n"
    "v_add_u32 v50, s65, v50\n"
    "v_xor_b32 v51, 64, v50\n"
    "v_add_u32 v52, 139392, v57\n"
    "v_lshl_or_b32 v53, v216, 2, v137\n"
    "v_lshl_or_b32 v53, v53, 2, v138\n"
    "v_lshlrev_b32 v53, 2, v53\n"
    "v_add_u32 v53, 139392, v53\n"
    "s_add_u32 m0, s55, 7168\n"
    "s_nop 0\n"
    "buffer_load_dwordx4 v49, s[36:39], s16 offen lds\n"
    "s_and_b32 s11, %[wg], 3\n"
    "s_mov_b32 s65, s52\n"
    "s_lshl_b32 s52, s11, 10\n"
    "s_add_u32 s52, s52, s65\n"
    "v_lshrrev_b32 v141, 2, v136\n"
    "v_lshl_add_u32 v58, v136, 10, s65\n"
    "s_add_u32 s59, s10, s58\n"
    "s_and_b32 s59, s59, 7\n"
    "s_lshl_b32 s53, s59, 10\n"
    "s_and_b32 s65, s59, 2\n"
    "s_lshl_b32 s65, s65, 2\n"
    "s_add_u32 s53, s53, s65\n"
    "s_and_b32 s65, s59, 4\n"
    "s_lshl_b32 s65, s65, 4\n"
    "s_add_u32 s53, s53, s65\n"
    "s_add_u32 s53, s53, 131072\n"
    "s_lshl_b32 s59, s59, 7\n"
    "s_xor_b32 s59, s59, s33\n"
    "s_lshl_b32 s59, s59, 9\n"
    "s_mul_i32 s57, s11, 3145728\n"
    "s_lshl_b32 s54, s10, 13\n"
    "s_lshr_b32 s60, %[wg], 3\n"
    "s_lshl_b32 s60, s60, 7\n"
    "s_and_b32 s65, %[wg], 4\n"
    "s_add_u32 s60, s60, s65\n"
    "s_lshl_b32 s60, s60, 7\n"
    "buffer_load_dwordx4 v[220:223], v204, s[4:7], s67 offen\n"
    "s_add_u32 m0, s55, 8192\n"
    "s_add_u32 s16, s56, 16384\n"
    "buffer_load_dwordx4 v48, s[36:39], s16 offen lds\n"
    "v_lshlrev_b32 v56, 11, v138\n"
    "v_and_b32 v143, 1, v136\n"
    "v_lshl_or_b32 v56, v143, 3, v56\n"
    "v_and_b32 v140, 2, v136\n"
    "v_lshl_or_b32 v56, v140, 5, v56\n"
    "v_lshl_or_b32 v56, v137, 7, v56\n"
    "v_and_b32 v142, 3, v141\n"
    "v_lshl_or_b32 v56, v142, 4, v56\n"
    "v_add_u32 v56, 131072, v56\n"
    "v_lshrrev_b32 v55, 1, v57\n"
    "v_lshrrev_b32 v142, 1, v136\n"
    "v_xor_b32 v142, v142, v136\n"
    "v_and_or_b32 v142, v142, 8, v140\n"
    "v_mov_b32 v140, 0x3f80\n"
    "v_cmp_eq_u32_e64 vcc, 0, v142\n"
    "v_cmp_eq_u32_e64 s[14:15], 2, v142\n"
    "v_and_b32 v141, 1, v141\n"
    "v_lshl_or_b32 v141, v143, 1, v141\n"
    "v_mul_u32_u24 v59, 0x5555, v141\n"
    "v_cndmask_b32_e64 v60, 0, v140, vcc\n"
    "v_cndmask_b32_e64 v61, 0, v140, s[14:15]\n"
    "v_cndmask_b32_e64 v62, 0, v140, vcc\n"
    "v_cndmask_b32_e64 v63, 0, v140, s[14:15]\n"
    "s_add_u32 m0, s55, 9216\n"
    "s_nop 0\n"
    "buffer_load_dwordx4 v49, s[36:39], s16 offen lds\n"
    "s_waitcnt lgkmcnt(0)\n"
    "s_and_b32 s65, s12, s30\n"
    "s_add_u32 s12, s12, 1\n"
    "s_mul_i32 s13, s12, 0x9e3779b1\n"
    "s_add_u32 s13, s13, 0x7f4a7c15\n"
    "s_mul_i32 s65, s65, 18874368\n"
    "s_add_u32 s65, s65, 1048576\n"
    "s_add_u32 s48, s26, s65\n"
    "s_addc_u32 s49, s27, 0\n"
    "s_add_u32 m0, s55, 10240\n"
    "s_add_u32 s16, s56, 20480\n"
    "buffer_load_dwordx4 v48, s[36:39], s16 offen lds\n"
    "s_waitcnt vmcnt(12)\n"
    "ds_read_b32 v0, v250 offset:384\n"
    "ds_read_b32 v8, v250 offset:784\n"
    "ds_read_b32 v16, v249\n"
    "ds_read_b32 v24, v249 offset:400\n"
    "ds_read_b32 v32, v249 offset:64\n"
    "ds_read_b32 v40, v249 offset:464\n"
    "ds_read_b32 v64, v249 offset:128\n"
    "ds_read_b32 v72, v249 offset:528\n"
    "ds_read_b32 v80, v249 offset:192\n"
    "ds_read_b32 v88, v249 offset:592\n"
    "ds_read_b32 v96, v249 offset:256\n"
    "ds_read_b32 v104, v249 offset:656\n"
    "ds_read_b32 v112, v249 offset:320\n"
    "ds_read_b32 v120, v249 offset:720\n"
    "s_add_u32 m0, s55, 11264\n"
    "s_nop 0\n"
    "buffer_load_dwordx4 v49, s[36:39], s16 offen lds\n"
    "s_add_u32 m0, s55, 12288\n"
    "s_add_u32 s16, s56, 24576\n"
    "buffer_load_dwordx4 v48, s[36:39], s16 offen lds\n"
    "s_add_u32 m0, s55, 13312\n"
    "s_nop 0\n"
    "buffer_load_dwordx4 v49, s[36:39], s16 offen lds\n"
    "s_add_u32 m0, s55, 14336\n"
    "s_add_u32 s16, s56, 28672\n"
    "buffer_load_dwordx4 v48, s[36:39], s16 offen lds\n"
    "s_add_u32 m0, s55, 15360\n"
    "s_nop 0\n"
    "buffer_load_dwordx4 v49, s[36:39], s16 offen lds\n"
    "s_lshr_b32 s55, s55, 2\n"
    "s_lshl_b32 s17, s55, 1\n"
    "s_add_u32 s17, s17, s33\n"
    "s_add_u32 m0, s55, 98304\n"
    "s_add_u32 s16, s17, 196608\n"
    "buffer_load_dwordx4 v48, s[36:39], s16 offen lds\n"
    "s_add_u32 m0, s55, 99328\n"
    "s_nop 0\n"
    "buffer_load_dwordx4 v49, s[36:39], s16 offen lds\n"
    "s_add_u32 m0, s55, 100352\n"
    "s_add_u32 s16, s17, 200704\n"
    "buffer_load_dwordx4 v48, s[36:39], s16 offen lds\n"
    "s_add_u32 m0, s55, 101376\n"
    "s_nop 0\n"
    "buffer_load_dwordx4 v49, s[36:39], s16 offen lds\n"
    "s_add_u32 m0, s55, 102400\n"
    "s_add_u32 s16, s17, 204800\n"
    "buffer_load_dwordx4 v48, s[36:39], s16 offen lds\n"
    "v_lshlrev_b32 v170, 4, v206\n"
    "s_mul_i32 s19, s64, 56\n"
    "s_add_u32 s19, s19, s58\n"
    "s_lshl3_add_u32 s19, s19, s63\n"
    "s_sub_u32 s65, s62, s64\n"
    "s_sub_u32 s65, s65, 1\n"
    "s_and_b32 s65, s65, 3\n"
    "s_lshl1_add_u32 s18, s65, s19\n"
    "s_lshl_b32 s66, s10, 7\n"
    "s_add_u32 s18, s18, s66\n"
    "s_lshl_b32 s18, s18, 9\n"
    "s_add_u32 s18, s18, 0\n"
    "s_lshl_b32 s19, s19, 9\n"
    "s_add_u32 s19, s19, 458752\n"
    "s_lshl2_add_u32 s65, s10, s65\n"
    "s_cmp_eq_u32 s65, 27\n"
    "s_cselect_b32 s19, s19, 2.0\n"
    "s_lshl3_add_u32 s65, s10, 130016\n"
    "s_cmp_lt_u32 s10, 6\n"
    "s_cselect_b32 s65, s65, 137472\n"
    "s_cselect_b32 s66, 0x100010, 0\n"
    "v_lshl_add_u32 v170, v168, 9, v170\n"
    "v_mul_u32_u24 v171, s66, v168\n"
    "v_lshl_add_u32 v170, v169, 2, v170\n"
    "v_add_u32 v170, s65, v170\n"
    "v_add_u32 v172, 0x300020, v171\n"
    "v_add_u32 v171, 0x100000, v171\n"
    "s_waitcnt vmcnt(13)\n"
    "v_lshlrev_b32 v188, 3, v220\n"
    "v_lshlrev_b32 v189, 11, v221\n"
    "v_and_b32 v188, 0x18, v188\n"
    "v_and_b32 v189, 0x1800, v189\n"
    "v_lshlrev_b32 v190, 19, v222\n"
    "v_lshlrev_b32 v191, 27, v223\n"
    "v_and_b32 v190, 0x180000, v190\n"
    "v_and_b32 v191, 0x18000000, v191\n"
    "v_or3_b32 v202, v188, v189, v190\n"
    "v_or_b32 v202, v202, v191\n"
    "s_nop 1\n"
    "v_mov_b32_dpp v188, v202 quad_perm:[0,0,0,0] row_mask:0xf bank_mask:0xf\n"
    "v_mov_b32_dpp v189, v202 quad_perm:[1,1,1,1] row_mask:0xf bank_mask:0xf\n"
    "v_mov_b32_dpp v232, v202 row_newbcast:2 row_mask:0xf bank_mask:0xf\n"
    "v_bfe_u32 v232, v232, v200, 5\n"
    "v_lshlrev_b32_e64 v184, v232, 3\n"
    "v_bfe_u32 v188, v188, v200, 5\n"
    "v_bfe_u32 v189, v189, v200, 5\n"
    "v_lshlrev_b32_e64 v188, v188, 3\n"
    "v_lshl_add_u32 v203, 3, v189, v188\n"
    "v_bfe_u32 v248, v203, v232, 5\n"
    "v_lshl_add_u32 v248, 4, v248, v201\n"
    "v_mov_b32_dpp v233, v202 row_newbcast:6 row_mask:0xf bank_mask:0xf\n"
    "v_bfe_u32 v233, v233, v200, 5\n"
    "v_add_u32_dpp v248, v248, v248 quad_perm:[1,0,3,2] row_mask:0xf bank_mask:0xf\n"
    "v_lshlrev_b32_e64 v185, v233, 3\n"
    "v_lshl_add_u32 v253, v220, 6, v171\n"
    "v_add_u32_dpp v248, v248, v248 quad_perm:[2,3,0,1] row_mask:0xf bank_mask:0xf\n"
    "s_waitcnt lgkmcnt(12)\n"
    "s_mov_b32 exec_lo, 0xeeeeeeee\n"
    "s_mov_b32 exec_hi, 0xeeeeeeee\n"
    "v_mov_b32 v0, 1275\n"
    "v_mov_b32 v8, 1275\n"
    "s_mov_b64 exec, -1\n"
    "v_min_u32_dpp v251, v248, v248 row_ror:4 row_mask:0xf bank_mask:0xf\n"
    "v_lshlrev_b32 v4, 3, v0\n"
    "v_bitop3_b32 v4, v4, v139, s31 bitop3:0x6c\n"
    "v_min_u32_dpp v252, v251, v251 row_ror:8 row_mask:0xf bank_mask:0xf\n"
    "v_cmp_eq_u32_e64 vcc, v252, v248\n"
    "v_lshl_or_b32 v0, v0, 7, v4\n"
    "v_xor_b32 v4, 64, v0\n"
    "v_cndmask_b32_e64 v184, 0, v184, vcc\n"
    "v_mov_b32_dpp v218, v252 quad_perm:[0,1,2,3] row_mask:0xf bank_mask:0x1\n"
    "v_add_u32 v203, v203, v184\n"
    "v_lshlrev_b32 v12, 3, v8\n"
    "v_bitop3_b32 v12, v12, v139, s31 bitop3:0x6c\n"
    "v_lshl_or_b32 v8, v8, 7, v12\n"
    "v_bfe_u32 v248, v203, v233, 5\n"
    "v_lshl_add_u32 v248, 4, v248, v201\n"
    "v_mov_b32_dpp v234, v202 row_newbcast:10 row_mask:0xf bank_mask:0xf\n"
    "v_bfe_u32 v234, v234, v200, 5\n"
    "v_add_u32_dpp v248, v248, v248 quad_perm:[1,0,3,2] row_mask:0xf bank_mask:0xf\n"
    "v_lshlrev_b32_e64 v186, v234, 3\n"
    "v_lshl_add_u32 v253, v221, 22, v253\n"
    "v_add_u32_dpp v248, v248, v248 quad_perm:[2,3,0,1] row_mask:0xf bank_mask:0xf\n"
    "v_xor_b32 v12, 64, v8\n"
    "s_waitcnt lgkmcnt(11)\n"
    "v_lshlrev_b32 v20, 3, v16\n"
    "v_min_u32_dpp v251, v248, v248 row_ror:4 row_mask:0xf bank_mask:0xf\n"
    "v_bitop3_b32 v20, v20, v139, s31 bitop3:0x6c\n"
    "v_lshl_or_b32 v16, v16, 7, v20\n"
    "v_min_u32_dpp v252, v251, v251 row_ror:8 row_mask:0xf bank_mask:0xf\n"
    "v_cmp_eq_u32_e64 vcc, v252, v248\n"
    "v_xor_b32 v20, 64, v16\n"
    "s_waitcnt lgkmcnt(10)\n"
    "v_lshlrev_b32 v28, 3, v24\n"
    "v_cndmask_b32_e64 v185, 0, v185, vcc\n"
    "v_mov_b32_dpp v218, v252 quad_perm:[0,1,2,3] row_mask:0xf bank_mask:0x2\n"
    "v_add_u32 v203, v203, v185\n"
    "v_bitop3_b32 v28, v28, v139, s31 bitop3:0x6c\n"
    "v_bfe_u32 v248, v203, v234, 5\n"
    "v_lshl_add_u32 v248, 4, v248, v201\n"
    "v_mov_b32_dpp v235, v202 row_newbcast:14 row_mask:0xf bank_mask:0xf\n"
    "v_bfe_u32 v235, v235, v200, 5\n"
    "v_add_u32_dpp v248, v248, v248 quad_perm:[1,0,3,2] row_mask:0xf bank_mask:0xf\n"
    "v_lshlrev_b32_e64 v187, v235, 3\n"
    "v_lshl_add_u32 v254, v222, 6, v172\n"
    "v_add_u32_dpp v248, v248, v248 quad_perm:[2,3,0,1] row_mask:0xf bank_mask:0xf\n"
    "v_lshl_or_b32 v24, v24, 7, v28\n"
    "v_xor_b32 v28, 64, v24\n"
    "v_min_u32_dpp v251, v248, v248 row_ror:4 row_mask:0xf bank_mask:0xf\n"
    "s_waitcnt lgkmcnt(9)\n"
    "v_lshlrev_b32 v36, 3, v32\n"
    "v_min_u32_dpp v252, v251, v251 row_ror:8 row_mask:0xf bank_mask:0xf\n"
    "v_cmp_eq_u32_e64 vcc, v252, v248\n"
    "v_bitop3_b32 v36, v36, v139, s31 bitop3:0x6c\n"
    "v_lshl_or_b32 v32, v32, 7, v36\n"
    "v_cndmask_b32_e64 v186, 0, v186, vcc\n"
    "v_mov_b32_dpp v218, v252 quad_perm:[0,1,2,3] row_mask:0xf bank_mask:0x4\n"
    "v_add_u32 v203, v203, v186\n"
    "v_xor_b32 v36, 64, v32\n"
    "s_waitcnt lgkmcnt(8)\n"
    "v_lshlrev_b32 v44, 3, v40\n"
    "v_bfe_u32 v248, v203, v235, 5\n"
    "v_lshl_add_u32 v248, 4, v248, v201\n"
    "v_mov_b32_dpp v236, v202 row_newbcast:3 row_mask:0xf bank_mask:0xf\n"
    "v_bfe_u32 v236, v236, v200, 5\n"
    "v_add_u32_dpp v248, v248, v248 quad_perm:[1,0,3,2] row_mask:0xf bank_mask:0xf\n"
    "v_lshlrev_b32_e64 v188, v236, 3\n"
    "v_lshl_add_u32 v254, v223, 22, v254\n"
    "v_add_u32_dpp v248, v248, v248 quad_perm:[2,3,0,1] row_mask:0xf bank_mask:0xf\n"
    "v_bitop3_b32 v44, v44, v139, s31 bitop3:0x6c\n"
    "v_lshl_or_b32 v40, v40, 7, v44\n"
    "v_min_u32_dpp v251, v248, v248 row_ror:4 row_mask:0xf bank_mask:0xf\n"
    "v_xor_b32 v44, 64, v40\n"
    "s_waitcnt lgkmcnt(7)\n"
    "v_lshlrev_b32 v68, 3, v64\n"
    "v_min_u32_dpp v252, v251, v251 row_ror:8 row_mask:0xf bank_mask:0xf\n"
    "v_cmp_eq_u32_e64 vcc, v252, v248\n"
    "v_bitop3_b32 v68, v68, v139, s31 bitop3:0x6c\n"
    "v_lshl_or_b32 v64, v64, 7, v68\n"
    "v_cndmask_b32_e64 v187, 0, v187, vcc\n"
    "v_mov_b32_dpp v218, v252 quad_perm:[0,1,2,3] row_mask:0xf bank_mask:0x8\n"
    "v_add_u32 v203, v203, v187\n"
    "v_xor_b32 v68, 64, v64\n"
    "v_bfe_u32 v248, v203, v236, 5\n"
    "v_lshl_add_u32 v248, 4, v248, v201\n"
    "v_mov_b32_dpp v237, v202 row_newbcast:7 row_mask:0xf bank_mask:0xf\n"
    "v_bfe_u32 v237, v237, v200, 5\n"
    "v_add_u32_dpp v248, v248, v248 quad_perm:[1,0,3,2] row_mask:0xf bank_mask:0xf\n"
    "v_lshlrev_b32_e64 v189, v237, 3\n"
    "s_waitcnt lgkmcnt(6)\n"
    "v_lshlrev_b32 v76, 3, v72\n"
    "v_add_u32_dpp v248, v248, v248 quad_perm:[2,3,0,1] row_mask:0xf bank_mask:0xf\n"
    "v_bitop3_b32 v76, v76, v139, s31 bitop3:0x6c\n"
    "v_lshl_or_b32 v72, v72, 7, v76\n"
    "v_min_u32_dpp v251, v248, v248 row_ror:4 row_mask:0xf bank_mask:0xf\n"
    "v_xor_b32 v76, 64, v72\n"
    "s_waitcnt lgkmcnt(5)\n"
    "v_lshlrev_b32 v84, 3, v80\n"
    "v_min_u32_dpp v252, v251, v251 row_ror:8 row_mask:0xf bank_mask:0xf\n"
    "v_cmp_eq_u32_e64 vcc, v252, v248\n"
    "v_bitop3_b32 v84, v84, v139, s31 bitop3:0x6c\n"
    "v_lshl_or_b32 v80, v80, 7, v84\n"
    "v_cndmask_b32_e64 v188, 0, v188, vcc\n"
    "v_mov_b32_dpp v219, v252 quad_perm:[0,1,2,3] row_mask:0xf bank_mask:0x1\n"
    "v_add_u32 v203, v203, v188\n"
    "v_bfe_u32 v248, v203, v237, 5\n"
    "v_lshl_add_u32 v248, 4, v248, v201\n"
    "v_mov_b32_dpp v238, v202 row_newbcast:11 row_mask:0xf bank_mask:0xf\n"
    "v_bfe_u32 v238, v238, v200, 5\n"
    "v_add_u32_dpp v248, v248, v248 quad_perm:[1,0,3,2] row_mask:0xf bank_mask:0xf\n"
    "v_lshlrev_b32_e64 v190, v238, 3\n"
    "v_xor_b32 v84, 64, v80\n"
    "v_add_u32_dpp v248, v248, v248 quad_perm:[2,3,0,1] row_mask:0xf bank_mask:0xf\n"
    "s_waitcnt lgkmcnt(4)\n"
    "v_lshlrev_b32 v92, 3, v88\n"
    "v_min_u32_dpp v251, v248, v248 row_ror:4 row_mask:0xf bank_mask:0xf\n"
    "v_bitop3_b32 v92, v92, v139, s31 bitop3:0x6c\n"
    "v_lshl_or_b32 v88, v88, 7, v92\n"
    "v_min_u32_dpp v252, v251, v251 row_ror:8 row_mask:0xf bank_mask:0xf\n"
    "v_cmp_eq_u32_e64 vcc, v252, v248\n"
    "v_xor_b32 v92, 64, v88\n"
    "s_waitcnt lgkmcnt(3)\n"
    "v_lshlrev_b32 v100, 3, v96\n"
    "v_cndmask_b32_e64 v189, 0, v189, vcc\n"
    "v_mov_b32_dpp v219, v252 quad_perm:[0,1,2,3] row_mask:0xf bank_mask:0x2\n"
    "v_add_u32 v203, v203, v189\n"
    "v_bitop3_b32 v100, v100, v139, s31 bitop3:0x6c\n"
    "v_bfe_u32 v248, v203, v238, 5\n"
    "v_lshl_add_u32 v248, 4, v248, v201\n"
    "v_mov_b32_dpp v239, v202 row_newbcast:15 row_mask:0xf bank_mask:0xf\n"
    "v_bfe_u32 v239, v239, v200, 5\n"
    "v_add_u32_dpp v248, v248, v248 quad_perm:[1,0,3,2] row_mask:0xf bank_mask:0xf\n"
    "v_lshlrev_b32_e64 v191, v239, 3\n"
    "v_lshl_or_b32 v96, v96, 7, v100\n"
    "v_add_u32_dpp v248, v248, v248 quad_perm:[2,3,0,1] row_mask:0xf bank_mask:0xf\n"
    "v_xor_b32 v100, 64, v96\n"
    "s_waitcnt lgkmcnt(2)\n"
    "v_lshlrev_b32 v108, 3, v104\n"
    "v_min_u32_dpp v251, v248, v248 row_ror:4 row_mask:0xf bank_mask:0xf\n"
    "v_bitop3_b32 v108, v108, v139, s31 bitop3:0x6c\n"
    "v_lshl_or_b32 v104, v104, 7, v108\n"
    "v_min_u32_dpp v252, v251, v251 row_ror:8 row_mask:0xf bank_mask:0xf\n"
    "v_cmp_eq_u32_e64 vcc, v252, v248\n"
    "v_xor_b32 v108, 64, v104\n"
    "s_waitcnt lgkmcnt(1)\n"
    "v_lshlrev_b32 v116, 3, v112\n"
    "v_cndmask_b32_e64 v190, 0, v190, vcc\n"
    "v_mov_b32_dpp v219, v252 quad_perm:[0,1,2,3] row_mask:0xf bank_mask:0x4\n"
    "v_add_u32 v203, v203, v190\n"
    "v_bfe_u32 v248, v203, v239, 5\n"
    "v_lshl_add_u32 v248, 4, v248, v201\n"
    "v_bitop3_b32 v116, v116, v139, s31 bitop3:0x6c\n"
    "v_lshl_or_b32 v112, v112, 7, v116\n"
    "v_add_u32_dpp v248, v248, v248 quad_perm:[1,0,3,2] row_mask:0xf bank_mask:0xf\n"
    "v_xor_b32 v116, 64, v112\n"
    "s_waitcnt lgkmcnt(0)\n"
    "v_lshlrev_b32 v124, 3, v120\n"
    "v_add_u32_dpp v248, v248, v248 quad_perm:[2,3,0,1] row_mask:0xf bank_mask:0xf\n"
    "v_bitop3_b32 v124, v124, v139, s31 bitop3:0x6c\n"
    "v_lshl_or_b32 v120, v120, 7, v124\n"
    "v_min_u32_dpp v251, v248, v248 row_ror:4 row_mask:0xf bank_mask:0xf\n"
    "v_xor_b32 v124, 64, v120\n"
    "s_nop 0\n"
    "v_min_u32_dpp v252, v251, v251 row_ror:8 row_mask:0xf bank_mask:0xf\n"
    "s_nop 1\n"
    "v_mov_b32_dpp v219, v252 quad_perm:[0,1,2,3] row_mask:0xf bank_mask:0x8\n"
    "v_cmp_eq_u32_e64 vcc, 16, v200\n"
    "v_cmp_eq_u32_e64 s[14:15], 24, v200\n"
    "s_nop 0\n"
    "v_cndmask_b32_e64 v216, v216, v218, vcc\n"
    "v_cndmask_b32_e64 v216, v216, v219, s[14:15]\n"
    "v_lshlrev_b32 v188, 4, v216\n"
    "v_xor_b32 v189, v216, v217\n"
    "v_and_b32 v189, 2, v189\n"
    "v_cmp_eq_u32_e64 vcc, 0, v189\n"
    "v_alignbit_b32 v253, v253, v253, v188\n"
    "v_alignbit_b32 v254, v254, v254, v188\n"
    "v_cndmask_b32_e64 v188, v254, v253, vcc\n"
    "v_cndmask_b32_e64 v189, v253, v254, vcc\n"
    "ds_write2_b32 v170, v188, v189 offset1:64\n"
    "s_nop 0\n"
    "v_permlane16_swap_b32 v188, v189\n"
    "v_mov_b32 v190, v188\n"
    "v_mov_b32 v191, v189\n"
    "v_mov_b32 v248, v188\n"
    "v_mov_b32 v251, v189\n"
    "s_nop 0\n"
    "v_permlane32_swap_b32 v248, v190\n"
    "v_permlane32_swap_b32 v251, v191\n"
    "s_mov_b32 exec_hi, 0\n"
    "buffer_store_dwordx4 v[188:191], v57, s[48:51], s19 offen sc1\n"
    "s_mov_b32 exec_hi, -1\n"
    "ds_read_b128 v[128:131], v50\n"
    "ds_read_b128 v[132:135], v51\n"
    "ds_read_b128 v[144:147], v50 offset:2048\n"
    "ds_read_b128 v[148:151], v51 offset:2048\n"
    "ds_read_b128 v[160:163], v50 offset:4096\n"
    "ds_read_b128 v[164:167], v51 offset:4096\n"
    "ds_read_b128 v[176:179], v50 offset:6144\n"
    "ds_read_b128 v[180:183], v51 offset:6144\n"
    "s_waitcnt vmcnt(12)\n"
    "ds_read_b128 v[192:195], v50 offset:8192\n"
    "ds_read_b128 v[196:199], v51 offset:8192\n"
    "s_waitcnt vmcnt(10)\n"
    "ds_read_b128 v[208:211], v50 offset:10240\n"
    "ds_read_b128 v[212:215], v51 offset:10240\n"
    "s_waitcnt vmcnt(8)\n"
    "ds_read_b128 v[224:227], v50 offset:12288\n"
    "ds_read_b128 v[228:231], v51 offset:12288\n"
    "s_waitcnt vmcnt(6)\n"
    "ds_read_b128 v[240:243], v50 offset:14336\n"
    "ds_read_b128 v[244:247], v51 offset:14336\n"
    "s_lshl_b32 s65, s10, 10\n"
    "s_add_u32 s65, s65, 123904\n"
    "s_movk_i32 s66, 32256\n"
    "v_add_u32 v170, s65, v57\n"
    "v_mad_u32_u24 v171, v169, s66, v57\n"
    "s_waitcnt lgkmcnt(15)\n"
    "s_waitcnt vmcnt(1)\n"
    "s_barrier\n"
    "s_setprio 3\n"
    "ds_read_b128 v[220:223], v170\n"
    "s_add_u32 s65, s57, s59\n"
    "s_add_u32 m0, s53, 0\n"
    "s_nop 0\n"
    "buffer_load_dwordx4 v58, s[44:47], s65 offen lds\n"
    "s_xor_b32 s65, s65, 262144\n"
    "s_bitcmp1_b32 s59, 18\n"
    "s_cselect_b32 s66, -8320, 0\n"
    "s_add_u32 s66, s66, s53\n"
    "s_add_u32 m0, s66, 4160\n"
    "s_nop 0\n"
    "buffer_load_dwordx4 v58, s[44:47], s65 offen lds\n"
    "ds_read_b128 v[0:3], v0\n"
    "ds_read_b128 v[8:11], v8\n"
    "s_waitcnt lgkmcnt(2)\n"
    "buffer_store_dwordx4 v[220:223], v171, s[48:51], s18 offen sc1\n"
    "s_add_u32 s65, s57, s59\n"
    "s_add_u32 s65, s65, 524288\n"
    "buffer_load_dwordx4 v[152:155], v58, s[44:47], s65 offen\n"
    "ds_read_b128 v[4:7], v4\n"
    "ds_read_b128 v[12:15], v12\n"
    "ds_read_b128 v[16:19], v16\n"
    "ds_read_b128 v[24:27], v24\n"
    "ds_read_b128 v[20:23], v20\n"
    "ds_read_b128 v[28:31], v28\n"
    "ds_read_b128 v[32:35], v32\n"
    "ds_read_b128 v[40:43], v40\n"
    "ds_read_b128 v[36:39], v36\n"
    "ds_read_b128 v[44:47], v44\n"
    "ds_read_b128 v[64:67], v64\n"
    "ds_read_b128 v[72:75], v72\n"
    "ds_read_b128 v[68:71], v68\n"
    "ds_read_b128 v[76:79], v76\n"
    "ds_read_b128 v[80:83], v80\n"
    "ds_read_b128 v[88:91], v88\n"
    "ds_read_b128 v[84:87], v84\n"
    "ds_read_b128 v[92:95], v92\n"
    "ds_read_b128 v[96:99], v96\n"
    "ds_read_b128 v[104:107], v104\n"
    "ds_read_b128 v[100:103], v100\n"
    "ds_read_b128 v[108:111], v108\n"
    "ds_read_b128 v[112:115], v112\n"
    "ds_read_b128 v[120:123], v120\n"
    "ds_read_b128 v[116:119], v116\n"
    "ds_read_b128 v[124:127], v124\n"
    "s_branch L_RB_%=\n"
    "L_q7_%=:\n"
    "s_and_b32 s63, %[wg], 1\n"
    "s_lshr_b32 s65, %[wg], 1\n"
    "s_lshl2_add_u32 s65, s65, s63\n"
    "s_lshl3_add_u32 s65, s65, s10\n"
    "s_mul_i32 s61, s65, 400\n"
    "s_mul_i32 s60, s10, 800\n"
    "s_add_u32 s60, s60, 131072\n"
    "v_cmp_gt_u32_e64 vcc, 25, v136\n"
    "v_add_u32 v209, 6000, v57\n"
    "v_lshrrev_b32 v137, 4, v136\n"
    "v_cndmask_b32_e64 v54, v209, v57, vcc\n"
    "s_bfe_u32 s58, %[wg], 0x30003\n"
    "s_lshr_b32 s64, %[wg], 6\n"
    "s_bfe_u32 s62, %[wg], 0x20001\n"
    "s_mov_b32 s6, 1638400\n"
    "s_mov_b32 s7, s15\n"
    "s_mov_b32 s38, 256000\n"
    "s_mov_b32 s39, s15\n"
    "s_mov_b32 s42, 20000\n"
    "s_mov_b32 s43, s15\n"
    "s_mov_b32 s46, 13107200\n"
    "s_mov_b32 s47, s15\n"
    "s_mov_b32 s50, 18874368\n"
    "s_mov_b32 s51, s15\n"
    "s_waitcnt lgkmcnt(0)\n"
    "s_mov_b64 s[4:5], s[76:77]\n"
    "s_mov_b64 s[36:37], s[78:79]\n"
    "s_mov_b64 s[40:41], s[80:81]\n"
    "s_mov_b64 s[26:27], s[90:91]\n"
    "s_mov_b64 s[44:45], s[82:83]\n"
    "s_mov_b64 s[20:21], s[84:85]\n"
    "s_mov_b64 s[22:23], s[86:87]\n"
    "s_mov_b64 s[24:25], s[88:89]\n"
    "s_load_dword s12, s[26:27], 0x0\n"
    "s_setprio 3\n"
    "s_mov_b32 m0, 139392\n"
    "s_movk_i32 s16, 0\n"
    "buffer_load_dwordx4 v57, s[40:43], s16 offen lds\n"
    "buffer_load_dwordx4 v57, s[40:43], s16 offen offset:1024 lds\n"
    "buffer_load_dwordx4 v57, s[40:43], s16 offen offset:2048 lds\n"
    "buffer_load_dwordx4 v57, s[40:43], s16 offen offset:3072 lds\n"
    "s_and_b32 s52, %[wg], 4\n"
    "s_add_u32 s52, s52, s64\n"
    "s_lshl3_add_u32 s52, s52, s58\n"
    "s_lshl_b32 s52, s52, 4\n"
    "s_load_dword s98, s[24:25], 0x0\n"
    "s_load_dwordx2 s[100:101], s[22:23], s52 offset:8\n"
    "s_load_dwordx2 s[24:25], s[22:23], s52\n"
    "s_load_dwordx4 s[20:23], s[20:21], s52\n"
    "s_load_dword s13, s[28:29], 0x0\n"
    "s_lshr_b32 s66, %[wg], 3\n"
    "s_add_u32 s53, s66, 768\n"
    "s_lshl_b32 s53, s53, 14\n"
    "s_load_dword s13, s[44:45], s53\n"
    "s_mov_b32 m0, 143488\n"
    "s_movk_i32 s16, 4096\n"
    "buffer_load_dwordx4 v57, s[40:43], s16 offen lds\n"
    "buffer_load_dwordx4 v57, s[40:43], s16 offen offset:1024 lds\n"
    "buffer_load_dwordx4 v57, s[40:43], s16 offen offset:2048 lds\n"
    "buffer_load_dwordx4 v57, s[40:43], s16 offen offset:3072 lds\n"
    "s_mov_b32 m0, 147584\n"
    "s_movk_i32 s16, 8192\n"
    "buffer_load_dwordx4 v57, s[40:43], s16 offen lds\n"
    "buffer_load_dwordx4 v57, s[40:43], s16 offen offset:1024 lds\n"
    "buffer_load_dwordx4 v57, s[40:43], s16 offen offset:2048 lds\n"
    "buffer_load_dwordx4 v57, s[40:43], s16 offen offset:3072 lds\n"
    "s_mov_b32 m0, 151680\n"
    "s_movk_i32 s16, 12288\n"
    "buffer_load_dwordx4 v57, s[40:43], s16 offen lds\n"
    "buffer_load_dwordx4 v57, s[40:43], s16 offen offset:1024 lds\n"
    "buffer_load_dwordx4 v57, s[40:43], s16 offen offset:2048 lds\n"
    "buffer_load_dwordx4 v57, s[40:43], s16 offen offset:3072 lds\n"
    "s_mov_b32 m0, 155776\n"
    "s_movk_i32 s16, 16384\n"
    "buffer_load_dwordx4 v57, s[40:43], s16 offen lds\n"
    "buffer_load_dwordx4 v57, s[40:43], s16 offen offset:1024 lds\n"
    "buffer_load_dwordx4 v57, s[40:43], s16 offen offset:2048 lds\n"
    "buffer_load_dwordx4 v57, s[40:43], s16 offen offset:3072 lds\n"
    "L_r7_%=:\n"
    "s_mov_b32 m0, s60\n"
    "s_mov_b32 exec_hi, 0x3ffff\n"
    "buffer_load_dwordx4 v54, s[4:7], s61 offen lds\n"
    "s_mov_b32 exec_hi, -1\n"
    "s_and_b32 s33, %[wg], 8\n"
    "s_lshl_b32 s55, s10, 14\n"
    "s_lshl_b32 s33, s33, 4\n"
    "s_lshl1_add_u32 s56, s55, s33\n"
    "v_bitop3_b32 v209, v136, v137, 2 bitop3:0x78\n"
    "v_lshrrev_b32 v208, 3, v136\n"
    "v_lshlrev_b32 v209, 4, v209\n"
    "v_lshl_add_u32 v48, v208, 7, v209\n"
    "v_xor_b32 v49, 0x840, v48\n"
    "v_and_b32 v138, 3, v136\n"
    "v_and_b32 v206, 15, v136\n"
    "v_lshrrev_b32 v216, 2, v206\n"
    "s_setprio 2\n"
    "v_mul_u32_u24 v251, 200, v206\n"
    "v_lshl_add_u32 v251, v137, 3, v251\n"
    "v_add_u32 v251, 139392, v251\n"
    "v_lshl_add_u32 v249, v206, 2, s60\n"
    "v_lshl_add_u32 v250, v216, 2, s60\n"
    "v_lshlrev_b32 v139, 4, v137\n"
    "s_movk_i32 s31, 96\n"
    "s_lshl_b32 s65, s10, 14\n"
    "v_lshl_or_b32 v209, v138, 2, v216\n"
    "v_lshlrev_b32 v208, 3, v209\n"
    "v_bitop3_b32 v208, v208, v139, s31 bitop3:0x6c\n"
    "v_lshl_or_b32 v50, v209, 7, v208\n"
    "v_add_u32 v50, s65, v50\n"
    "v_xor_b32 v51, 64, v50\n"
    "v_add_u32 v52, 139392, v57\n"
    "v_lshl_or_b32 v53, v216, 2, v137\n"
    "v_lshl_or_b32 v53, v53, 2, v138\n"
    "v_lshlrev_b32 v53, 2, v53\n"
    "v_add_u32 v53, 139392, v53\n"
    "s_and_b32 s11, %[wg], 3\n"
    "s_mov_b32 s65, s52\n"
    "s_lshl_b32 s52, s11, 10\n"
    "s_add_u32 s52, s52, s65\n"
    "v_lshrrev_b32 v141, 2, v136\n"
    "v_lshl_add_u32 v58, v136, 10, s65\n"
    "s_add_u32 s59, s10, s58\n"
    "s_and_b32 s59, s59, 7\n"
    "s_lshl_b32 s53, s59, 10\n"
    "s_and_b32 s65, s59, 2\n"
    "s_lshl_b32 s65, s65, 2\n"
    "s_add_u32 s53, s53, s65\n"
    "s_and_b32 s65, s59, 4\n"
    "s_lshl_b32 s65, s65, 4\n"
    "s_add_u32 s53, s53, s65\n"
    "s_add_u32 s53, s53, 131072\n"
    "s_lshl_b32 s59, s59, 7\n"
    "s_xor_b32 s59, s59, s33\n"
    "s_lshl_b32 s59, s59, 9\n"
    "s_mul_i32 s57, s11, 3145728\n"
    "s_lshl_b32 s54, s10, 13\n"
    "s_lshr_b32 s60, %[wg], 3\n"
    "s_lshl_b32 s60, s60, 7\n"
    "s_and_b32 s65, %[wg], 4\n"
    "s_add_u32 s60, s60, s65\n"
    "s_lshl_b32 s60, s60, 7\n"
    "v_lshlrev_b32 v56, 11, v138\n"
    "v_and_b32 v143, 1, v136\n"
    "v_lshl_or_b32 v56, v143, 3, v56\n"
    "v_and_b32 v140, 2, v136\n"
    "v_lshl_or_b32 v56, v140, 5, v56\n"
    "v_lshl_or_b32 v56, v137, 7, v56\n"
    "v_and_b32 v142, 3, v141\n"
    "v_lshl_or_b32 v56, v142, 4, v56\n"
    "v_add_u32 v56, 131072, v56\n"
    "v_lshrrev_b32 v55, 1, v57\n"
    "v_lshrrev_b32 v142, 1, v136\n"
    "v_xor_b32 v142, v142, v136\n"
    "v_and_or_b32 v142, v142, 8, v140\n"
    "v_mov_b32 v140, 0x3f80\n"
    "v_cmp_eq_u32_e64 vcc, 0, v142\n"
    "v_cmp_eq_u32_e64 s[14:15], 2, v142\n"
    "v_and_b32 v141, 1, v141\n"
    "v_lshl_or_b32 v141, v143, 1, v141\n"
    "v_mul_u32_u24 v59, 0x5555, v141\n"
    "v_cndmask_b32_e64 v60, 0, v140, vcc\n"
    "v_cndmask_b32_e64 v61, 0, v140, s[14:15]\n"
    "v_cndmask_b32_e64 v62, 0, v140, vcc\n"
    "v_cndmask_b32_e64 v63, 0, v140, s[14:15]\n"
    "s_waitcnt lgkmcnt(0)\n"
    "s_and_b32 s65, s12, s30\n"
    "s_add_u32 s12, s12, 1\n"
    "s_mul_i32 s13, s12, 0x9e3779b1\n"
    "s_add_u32 s13, s13, 0x7f4a7c15\n"
    "s_mul_i32 s65, s65, 18874368\n"
    "s_add_u32 s65, s65, 1048576\n"
    "s_add_u32 s48, s26, s65\n"
    "s_addc_u32 s49, s27, 0\n"
    "s_waitcnt vmcnt(1)\n"
    "ds_read_b64 v[128:129], v251 offset:192\n"
    "ds_read_b64 v[130:131], v251 offset:19200\n"
    "ds_read_b64 v[132:133], v251 offset:3392\n"
    "ds_read_b64 v[134:135], v251 offset:19232\n"
    "ds_read_b64 v[144:145], v251 offset:6592\n"
    "ds_read_b64 v[146:147], v251 offset:19264\n"
    "ds_read_b64 v[148:149], v251 offset:9792\n"
    "ds_read_b64 v[150:151], v251 offset:19296\n"
    "ds_read_b64 v[152:153], v251 offset:12992\n"
    "ds_read_b64 v[154:155], v251 offset:19328\n"
    "ds_read_b64 v[156:157], v251 offset:16192\n"
    "ds_read_b64 v[158:159], v251 offset:19360\n"
    "ds_read_b64 v[160:161], v251 offset:19392\n"
    "ds_read_b64 v[162:163], v251\n"
    "ds_read_b64 v[164:165], v251 offset:32\n"
    "ds_read_b64 v[166:167], v251 offset:3200\n"
    "ds_read_b64 v[168:169], v251 offset:3232\n"
    "ds_read_b64 v[170:171], v251 offset:64\n"
    "ds_read_b64 v[172:173], v251 offset:6400\n"
    "ds_read_b64 v[174:175], v251 offset:3264\n"
    "ds_read_b64 v[176:177], v251 offset:6432\n"
    "ds_read_b64 v[178:179], v251 offset:6464\n"
    "ds_read_b64 v[180:181], v251 offset:96\n"
    "ds_read_b64 v[182:183], v251 offset:9600\n"
    "ds_read_b64 v[184:185], v251 offset:3296\n"
    "ds_read_b64 v[186:187], v251 offset:9632\n"
    "ds_read_b64 v[188:189], v251 offset:6496\n"
    "ds_read_b64 v[190:191], v251 offset:9664\n"
    "ds_read_b64 v[192:193], v251 offset:9696\n"
    "ds_read_b64 v[194:195], v251 offset:128\n"
    "ds_read_b64 v[196:197], v251 offset:12800\n"
    "ds_read_b64 v[198:199], v251 offset:3328\n"
    "ds_read_b64 v[200:201], v251 offset:12832\n"
    "ds_read_b64 v[66:67], v251 offset:6528\n"
    "ds_read_b64 v[70:71], v251 offset:12864\n"
    "ds_read_b64 v[74:75], v251 offset:9728\n"
    "ds_read_b64 v[78:79], v251 offset:12896\n"
    "ds_read_b64 v[82:83], v251 offset:12928\n"
    "ds_read_b64 v[86:87], v251 offset:160\n"
    "ds_read_b64 v[90:91], v251 offset:16000\n"
    "ds_read_b64 v[94:95], v251 offset:3360\n"
    "ds_read_b64 v[98:99], v251 offset:16032\n"
    "ds_read_b64 v[102:103], v251 offset:6560\n"
    "ds_read_b64 v[106:107], v251 offset:16064\n"
    "ds_read_b64 v[110:111], v251 offset:9760\n"
    "ds_read_b64 v[114:115], v251 offset:16096\n"
    "ds_read_b64 v[118:119], v251 offset:12960\n"
    "ds_read_b64 v[122:123], v251 offset:16128\n"
    "ds_read_b64 v[126:127], v251 offset:16160\n"
    "v_mov_b32 v210, 0x3f80\n"
    "v_cmp_eq_u32_e64 vcc, v216, v137\n"
    "v_mov_b32 v213, 0\n"
    "v_and_b32 v214, 0x33, v136\n"
    "v_lshlrev_b32 v211, 4, v138\n"
    "v_cndmask_b32_e64 v212, 0, v210, vcc\n"
    "v_lshlrev_b64 v[252:253], v211, v[212:213]\n"
    "v_cmp_eq_u32_e64 vcc, 0, v214\n"
    "v_lshlrev_b32 v211, 4, v216\n"
    "s_nop 0\n"
    "v_cndmask_b32_e64 v212, 0, v210, vcc\n"
    "v_lshlrev_b64 v[254:255], v211, v[212:213]\n"
    "v_cmp_gt_u32_e64 vcc, 16, v136\n"
    "s_and_b32 s66, %[wg], 63\n"
    "s_min_u32 s65, s66, 22\n"
    "s_lshl_b32 s18, s65, 10\n"
    "s_lshr_b32 s65, s65, 3\n"
    "s_lshl_b32 s65, s65, 7\n"
    "s_add_u32 s34, s18, s65\n"
    "s_mul_i32 s65, s64, 24576\n"
    "s_add_u32 s18, s18, s65\n"
    "s_add_u32 s18, s18, 65536\n"
    "s_cmp_lt_u32 s66, 23\n"
    "s_cselect_b32 s18, s18, 1073741824\n"
    "v_lshlrev_b32 v204, 2, v136\n"
    "v_add_u32 v204, 163200, v204\n"
    "ds_write_b32 v204, v213\n"
    "v_mov_b32 v204, 163584\n"
    "ds_write_b32 v204, v213\n"
    "s_waitcnt vmcnt(0)\n"
    "ds_read_b32 v0, v250 offset:384\n"
    "ds_read_b32 v8, v250 offset:784\n"
    "ds_read_b32 v16, v249\n"
    "ds_read_b32 v24, v249 offset:400\n"
    "ds_read_b32 v32, v249 offset:64\n"
    "ds_read_b32 v40, v249 offset:464\n"
    "ds_read_b32 v64, v249 offset:128\n"
    "ds_read_b32 v72, v249 offset:528\n"
    "ds_read_b32 v80, v249 offset:192\n"
    "ds_read_b32 v88, v249 offset:592\n"
    "ds_read_b32 v96, v249 offset:256\n"
    "ds_read_b32 v104, v249 offset:656\n"
    "ds_read_b32 v112, v249 offset:320\n"
    "ds_read_b32 v120, v249 offset:720\n"
    "s_waitcnt lgkmcnt(15)\n"
    "v_mfma_f32_16x16x16_bf16 v[208:211], v[252:253], v[130:131], 0\n"
    "v_mfma_f32_16x16x16_bf16 v[208:211], v[128:129], v[252:253], v[208:211]\n"
    "s_waitcnt lgkmcnt(15)\n"
    "v_mfma_f32_16x16x16_bf16 v[212:215], v[252:253], v[134:135], 0\n"
    "v_mfma_f32_16x16x16_bf16 v[212:215], v[132:133], v[252:253], v[212:215]\n"
    "s_waitcnt lgkmcnt(15)\n"
    "v_mfma_f32_16x16x16_bf16 v[216:219], v[252:253], v[146:147], 0\n"
    "v_mfma_f32_16x16x16_bf16 v[216:219], v[144:145], v[252:253], v[216:219]\n"
    "s_waitcnt lgkmcnt(15)\n"
    "v_mfma_f32_16x16x16_bf16 v[220:223], v[252:253], v[150:151], 0\n"
    "v_mfma_f32_16x16x16_bf16 v[220:223], v[148:149], v[252:253], v[220:223]\n"
    "s_waitcnt lgkmcnt(15)\n"
    "v_mfma_f32_16x16x16_bf16 v[224:227], v[252:253], v[154:155], 0\n"
    "v_mfma_f32_16x16x16_bf16 v[224:227], v[152:153], v[252:253], v[224:227]\n"
    "s_waitcnt lgkmcnt(15)\n"
    "v_mfma_f32_16x16x16_bf16 v[228:231], v[252:253], v[158:159], 0\n"
    "v_mfma_f32_16x16x16_bf16 v[228:231], v[156:157], v[252:253], v[228:231]\n"
    "s_waitcnt lgkmcnt(15)\n"
    "v_cndmask_b32_e64 v160, 0, v160, vcc\n"
    "v_cndmask_b32_e64 v161, 0, v161, vcc\n"
    "s_nop 1\n"
    "v_mfma_f32_16x16x16_bf16 v[232:235], v[254:255], v[160:161], 0\n"
    "s_waitcnt lgkmcnt(15)\n"
    "v_mfma_f32_16x16x16_bf16 v[236:239], v[252:253], v[162:163], 0\n"
    "s_waitcnt lgkmcnt(15)\n"
    "v_mfma_f32_16x16x16_bf16 v[240:243], v[252:253], v[166:167], 0\n"
    "v_mfma_f32_16x16x16_bf16 v[240:243], v[164:165], v[252:253], v[240:243]\n"
    "s_waitcnt lgkmcnt(0)\n"
    "v_and_or_b32 v205, v136, 12, v137\n"
    "v_lshl_or_b32 v205, v205, 2, v138\n"
    "v_lshlrev_b32 v205, 4, v205\n"
    "v_add_u32 v251, 139392, v205\n"
    "v_mov_b32_dpp v208, v212 row_shr:4 row_mask:0xf bank_mask:0x2\n"
    "v_mov_b32_dpp v209, v213 row_shr:4 row_mask:0xf bank_mask:0x2\n"
    "v_mov_b32_dpp v210, v214 row_shr:4 row_mask:0xf bank_mask:0x2\n"
    "v_mov_b32_dpp v211, v215 row_shr:4 row_mask:0xf bank_mask:0x2\n"
    "v_mov_b32_dpp v208, v216 row_shr:8 row_mask:0xf bank_mask:0x4\n"
    "v_mov_b32_dpp v209, v217 row_shr:8 row_mask:0xf bank_mask:0x4\n"
    "v_mov_b32_dpp v210, v218 row_shr:8 row_mask:0xf bank_mask:0x4\n"
    "v_mov_b32_dpp v211, v219 row_shr:8 row_mask:0xf bank_mask:0x4\n"
    "v_mov_b32_dpp v208, v220 row_shr:12 row_mask:0xf bank_mask:0x8\n"
    "v_mov_b32_dpp v209, v221 row_shr:12 row_mask:0xf bank_mask:0x8\n"
    "v_mov_b32_dpp v210, v222 row_shr:12 row_mask:0xf bank_mask:0x8\n"
    "v_mov_b32_dpp v211, v223 row_shr:12 row_mask:0xf bank_mask:0x8\n"
    "ds_write_b128 v251, v[208:211]\n"
    "v_mov_b32_dpp v224, v228 row_shr:4 row_mask:0xf bank_mask:0x2\n"
    "v_mov_b32_dpp v225, v229 row_shr:4 row_mask:0xf bank_mask:0x2\n"
    "v_mov_b32_dpp v226, v230 row_shr:4 row_mask:0xf bank_mask:0x2\n"
    "v_mov_b32_dpp v227, v231 row_shr:4 row_mask:0xf bank_mask:0x2\n"
    "v_mov_b32_dpp v224, v232 row_shr:8 row_mask:0xf bank_mask:0x4\n"
    "v_mov_b32_dpp v225, v233 row_shr:8 row_mask:0xf bank_mask:0x4\n"
    "v_mov_b32_dpp v226, v234 row_shr:8 row_mask:0xf bank_mask:0x4\n"
    "v_mov_b32_dpp v227, v235 row_shr:8 row_mask:0xf bank_mask:0x4\n"
    "ds_write_b128 v251, v[224:227] offset:1024\n"
    "v_mfma_f32_16x16x16_bf16 v[244:247], v[252:253], v[168:169], 0\n"
    "s_mov_b32 exec_lo, 0xeeeeeeee\n"
    "s_mov_b32 exec_hi, 0xeeeeeeee\n"
    "v_mov_b32 v0, 1275\n"
    "v_mov_b32 v8, 1275\n"
    "s_mov_b64 exec, -1\n"
    "v_mfma_f32_16x16x16_bf16 v[208:211], v[252:253], v[172:173], 0\n"
    "v_lshlrev_b32 v4, 3, v0\n"
    "v_mfma_f32_16x16x16_bf16 v[208:211], v[170:171], v[252:253], v[208:211]\n"
    "v_bitop3_b32 v4, v4, v139, s31 bitop3:0x6c\n"
    "ds_write_b128 v52, v[236:239] offset:2048\n"
    "v_mfma_f32_16x16x16_bf16 v[212:215], v[252:253], v[176:177], 0\n"
    "v_lshl_or_b32 v0, v0, 7, v4\n"
    "v_mfma_f32_16x16x16_bf16 v[212:215], v[174:175], v[252:253], v[212:215]\n"
    "v_xor_b32 v4, 64, v0\n"
    "ds_write_b128 v52, v[240:243] offset:3072\n"
    "v_mfma_f32_16x16x16_bf16 v[216:219], v[252:253], v[178:179], 0\n"
    "v_lshlrev_b32 v12, 3, v8\n"
    "ds_write_b128 v52, v[244:247] offset:4096\n"
    "v_mfma_f32_16x16x16_bf16 v[220:223], v[252:253], v[182:183], 0\n"
    "v_bitop3_b32 v12, v12, v139, s31 bitop3:0x6c\n"
    "v_mfma_f32_16x16x16_bf16 v[220:223], v[180:181], v[252:253], v[220:223]\n"
    "v_lshl_or_b32 v8, v8, 7, v12\n"
    "ds_write_b128 v52, v[208:211] offset:5120\n"
    "v_mfma_f32_16x16x16_bf16 v[224:227], v[252:253], v[186:187], 0\n"
    "v_xor_b32 v12, 64, v8\n"
    "v_mfma_f32_16x16x16_bf16 v[224:227], v[184:185], v[252:253], v[224:227]\n"
    "v_lshlrev_b32 v20, 3, v16\n"
    "ds_write_b128 v52, v[212:215] offset:6144\n"
    "v_mfma_f32_16x16x16_bf16 v[228:231], v[252:253], v[190:191], 0\n"
    "v_bitop3_b32 v20, v20, v139, s31 bitop3:0x6c\n"
    "v_mfma_f32_16x16x16_bf16 v[228:231], v[188:189], v[252:253], v[228:231]\n"
    "v_lshl_or_b32 v16, v16, 7, v20\n"
    "ds_write_b128 v52, v[216:219] offset:7168\n"
    "v_mfma_f32_16x16x16_bf16 v[232:235], v[252:253], v[192:193], 0\n"
    "v_xor_b32 v20, 64, v16\n"
    "ds_write_b128 v52, v[220:223] offset:8320\n"
    "v_mfma_f32_16x16x16_bf16 v[236:239], v[252:253], v[196:197], 0\n"
    "v_lshlrev_b32 v28, 3, v24\n"
    "v_mfma_f32_16x16x16_bf16 v[236:239], v[194:195], v[252:253], v[236:239]\n"
    "v_bitop3_b32 v28, v28, v139, s31 bitop3:0x6c\n"
    "ds_write_b128 v52, v[224:227] offset:9344\n"
    "v_mfma_f32_16x16x16_bf16 v[240:243], v[252:253], v[200:201], 0\n"
    "v_lshl_or_b32 v24, v24, 7, v28\n"
    "v_mfma_f32_16x16x16_bf16 v[240:243], v[198:199], v[252:253], v[240:243]\n"
    "v_xor_b32 v28, 64, v24\n"
    "ds_write_b128 v52, v[228:231] offset:10368\n"
    "v_mfma_f32_16x16x16_bf16 v[244:247], v[252:253], v[70:71], 0\n"
    "v_lshlrev_b32 v36, 3, v32\n"
    "v_mfma_f32_16x16x16_bf16 v[244:247], v[66:67], v[252:253], v[244:247]\n"
    "v_bitop3_b32 v36, v36, v139, s31 bitop3:0x6c\n"
    "ds_write_b128 v52, v[232:235] offset:11392\n"
    "v_mfma_f32_16x16x16_bf16 v[208:211], v[252:253], v[78:79], 0\n"
    "v_lshl_or_b32 v32, v32, 7, v36\n"
    "v_mfma_f32_16x16x16_bf16 v[208:211], v[74:75], v[252:253], v[208:211]\n"
    "v_xor_b32 v36, 64, v32\n"
    "ds_write_b128 v52, v[236:239] offset:12416\n"
    "s_waitcnt lgkmcnt(0)\n"
    "s_barrier\n"
    "s_setprio 2\n"
    "s_add_u32 s65, s57, s59\n"
    "s_add_u32 m0, s53, 0\n"
    "s_nop 0\n"
    "buffer_load_dwordx4 v58, s[44:47], s65 offen lds\n"
    "s_xor_b32 s65, s65, 262144\n"
    "s_bitcmp1_b32 s59, 18\n"
    "s_cselect_b32 s66, -8320, 0\n"
    "s_add_u32 s66, s66, s53\n"
    "s_add_u32 m0, s66, 4160\n"
    "s_nop 0\n"
    "buffer_load_dwordx4 v58, s[44:47], s65 offen lds\n"
    "v_mfma_f32_16x16x16_bf16 v[212:215], v[252:253], v[82:83], 0\n"
    "v_lshlrev_b32 v44, 3, v40\n"
    "ds_write_b128 v52, v[240:243] offset:13440\n"
    "v_mfma_f32_16x16x16_bf16 v[216:219], v[252:253], v[90:91], 0\n"
    "v_bitop3_b32 v44, v44, v139, s31 bitop3:0x6c\n"
    "v_mfma_f32_16x16x16_bf16 v[216:219], v[86:87], v[252:253], v[216:219]\n"
    "v_lshl_or_b32 v40, v40, 7, v44\n"
    "ds_write_b128 v52, v[244:247] offset:14464\n"
    "v_mfma_f32_16x16x16_bf16 v[220:223], v[252:253], v[98:99], 0\n"
    "v_xor_b32 v44, 64, v40\n"
    "v_mfma_f32_16x16x16_bf16 v[220:223], v[94:95], v[252:253], v[220:223]\n"
    "v_lshlrev_b32 v68, 3, v64\n"
    "ds_write_b128 v52, v[208:211] offset:15488\n"
    "v_mfma_f32_16x16x16_bf16 v[224:227], v[252:253], v[106:107], 0\n"
    "v_bitop3_b32 v68, v68, v139, s31 bitop3:0x6c\n"
    "v_mfma_f32_16x16x16_bf16 v[224:227], v[102:103], v[252:253], v[224:227]\n"
    "v_lshl_or_b32 v64, v64, 7, v68\n"
    "ds_write_b128 v52, v[212:215] offset:16640\n"
    "v_mfma_f32_16x16x16_bf16 v[228:231], v[252:253], v[114:115], 0\n"
    "v_xor_b32 v68, 64, v64\n"
    "v_mfma_f32_16x16x16_bf16 v[228:231], v[110:111], v[252:253], v[228:231]\n"
    "v_lshlrev_b32 v76, 3, v72\n"
    "ds_write_b128 v52, v[216:219] offset:17664\n"
    "v_mfma_f32_16x16x16_bf16 v[232:235], v[252:253], v[122:123], 0\n"
    "v_bitop3_b32 v76, v76, v139, s31 bitop3:0x6c\n"
    "v_mfma_f32_16x16x16_bf16 v[232:235], v[118:119], v[252:253], v[232:235]\n"
    "v_lshl_or_b32 v72, v72, 7, v76\n"
    "ds_write_b128 v52, v[220:223] offset:18688\n"
    "v_mfma_f32_16x16x16_bf16 v[236:239], v[252:253], v[126:127], 0\n"
    "v_xor_b32 v76, 64, v72\n"
    "ds_write_b128 v52, v[224:227] offset:19712\n"
    "v_lshlrev_b32 v84, 3, v80\n"
    "v_bitop3_b32 v84, v84, v139, s31 bitop3:0x6c\n"
    "v_lshl_or_b32 v80, v80, 7, v84\n"
    "v_xor_b32 v84, 64, v80\n"
    "v_lshlrev_b32 v92, 3, v88\n"
    "v_bitop3_b32 v92, v92, v139, s31 bitop3:0x6c\n"
    "v_lshl_or_b32 v88, v88, 7, v92\n"
    "v_xor_b32 v92, 64, v88\n"
    "v_lshlrev_b32 v100, 3, v96\n"
    "v_bitop3_b32 v100, v100, v139, s31 bitop3:0x6c\n"
    "v_lshl_or_b32 v96, v96, 7, v100\n"
    "v_xor_b32 v100, 64, v96\n"
    "v_lshlrev_b32 v108, 3, v104\n"
    "v_bitop3_b32 v108, v108, v139, s31 bitop3:0x6c\n"
    "v_lshl_or_b32 v104, v104, 7, v108\n"
    "v_xor_b32 v108, 64, v104\n"
    "v_lshlrev_b32 v116, 3, v112\n"
    "v_bitop3_b32 v116, v116, v139, s31 bitop3:0x6c\n"
    "v_lshl_or_b32 v112, v112, 7, v116\n"
    "v_xor_b32 v116, 64, v112\n"
    "v_lshlrev_b32 v124, 3, v120\n"
    "v_bitop3_b32 v124, v124, v139, s31 bitop3:0x6c\n"
    "v_lshl_or_b32 v120, v120, 7, v124\n"
    "v_xor_b32 v124, 64, v120\n"
    "ds_write_b128 v52, v[228:231] offset:20736\n"
    "ds_write_b128 v52, v[232:235] offset:21760\n"
    "ds_write_b128 v52, v[236:239] offset:22784\n"
    "ds_write_b32 v204, v204\n"
    "v_add_u32 v204, s34, v52\n"
    "ds_read_b128 v[204:207], v204\n"
    "s_waitcnt lgkmcnt(0)\n"
    "buffer_store_dwordx4 v[204:207], v57, s[48:51], s18 offen sc1\n"
    "s_setprio 2\n"
    "ds_read_b128 v[128:131], v50\n"
    "ds_read_b128 v[132:135], v51\n"
    "ds_read_b128 v[144:147], v50 offset:2048\n"
    "ds_read_b128 v[148:151], v51 offset:2048\n"
    "ds_read_b128 v[160:163], v50 offset:4096\n"
    "ds_read_b128 v[164:167], v51 offset:4096\n"
    "ds_read_b128 v[176:179], v50 offset:6144\n"
    "ds_read_b128 v[180:183], v51 offset:6144\n"
    "ds_read_b128 v[192:195], v50 offset:8192\n"
    "ds_read_b128 v[196:199], v51 offset:8192\n"
    "ds_read_b128 v[208:211], v50 offset:10240\n"
    "ds_read_b128 v[212:215], v51 offset:10240\n"
    "ds_read_b128 v[224:227], v50 offset:12288\n"
    "ds_read_b128 v[228:231], v51 offset:12288\n"
    "ds_read_b128 v[240:243], v50 offset:14336\n"
    "ds_read_b128 v[244:247], v51 offset:14336\n"
    "ds_read_b128 v[0:3], v0\n"
    "ds_read_b128 v[8:11], v8\n"
    "ds_read_b128 v[4:7], v4\n"
    "ds_read_b128 v[12:15], v12\n"
    "ds_read_b128 v[16:19], v16\n"
    "ds_read_b128 v[24:27], v24\n"
    "ds_read_b128 v[20:23], v20\n"
    "ds_read_b128 v[28:31], v28\n"
    "ds_read_b128 v[32:35], v32\n"
    "ds_read_b128 v[40:43], v40\n"
    "ds_read_b128 v[36:39], v36\n"
    "ds_read_b128 v[44:47], v44\n"
    "ds_read_b128 v[64:67], v64\n"
    "ds_read_b128 v[72:75], v72\n"
    "ds_read_b128 v[68:71], v68\n"
    "ds_read_b128 v[76:79], v76\n"
    "ds_read_b128 v[80:83], v80\n"
    "ds_read_b128 v[88:91], v88\n"
    "ds_read_b128 v[84:87], v84\n"
    "ds_read_b128 v[92:95], v92\n"
    "ds_read_b128 v[96:99], v96\n"
    "ds_read_b128 v[104:107], v104\n"
    "ds_read_b128 v[100:103], v100\n"
    "ds_read_b128 v[108:111], v108\n"
    "ds_read_b128 v[112:115], v112\n"
    "ds_read_b128 v[120:123], v120\n"
    "ds_read_b128 v[116:119], v116\n"
    "ds_read_b128 v[124:127], v124\n"
    "s_add_u32 s65, s57, s59\n"
    "s_add_u32 s65, s65, 524288\n"
    "buffer_load_dwordx4 v[152:155], v58, s[44:47], s65 offen\n"
    "L_RB_%=:\n"
    "s_setprio 1\n"
    "s_sub_u32 s56, s56, s33\n"
    "s_xor_b32 s33, s33, 128\n"
    "s_add_u32 s56, s56, s33\n"
    "s_add_u32 s55, s54, 8188\n"
    "s_lshl_b32 s18, s52, 12\n"
    "s_add_u32 s18, s18, s54\n"
    "s_add_u32 s18, s18, 2097152\n"
    "s_add_u32 s19, s18, 4096\n"
    "s_mul_i32 s34, s11, 24576\n"
    "s_lshl_b32 s35, s10, 10\n"
    "s_add_u32 s34, s34, s35\n"
    "s_add_u32 s34, s34, 65536\n"
    "s_cmp_lt_u32 s10, 4\n"
    "s_cselect_b64 vcc, -1, 0\n"
    "v_subrev_u32 v50, s54, v50\n"
    "v_subrev_u32 v51, s54, v51\n"
    "s_setprio 1\n"
    "s_waitcnt lgkmcnt(15)\n"
    "v_mfma_f32_16x16x32_bf16 v[248:251], v[0:3], v[0:3], 0\n"
    "ds_read_b32 v200, v53 offset:1536\n"
    "s_waitcnt lgkmcnt(15)\n"
    "v_mfma_f32_16x16x32_bf16 v[248:251], v[4:7], v[4:7], v[248:251]\n"
    "v_mfma_f32_16x16x32_bf16 v[252:255], v[8:11], v[8:11], 0\n"
    "s_waitcnt lgkmcnt(15)\n"
    "v_mfma_f32_16x16x32_bf16 v[252:255], v[12:15], v[12:15], v[252:255]\n"
    "s_waitcnt lgkmcnt(15)\n"
    "v_mfma_f32_16x16x32_bf16 v[232:235], v[0:3], v[16:19], 0\n"
    "ds_read_b32 v204, v53\n"
    "s_waitcnt lgkmcnt(15)\n"
    "v_mfma_f32_16x16x32_bf16 v[232:235], v[4:7], v[20:23], v[232:235]\n"
    "v_mfma_f32_16x16x32_bf16 v[236:239], v[8:11], v[24:27], 0\n"
    "s_waitcnt lgkmcnt(15)\n"
    "v_mfma_f32_16x16x32_bf16 v[236:239], v[12:15], v[28:31], v[236:239]\n"
    "s_waitcnt vmcnt(2)\n"
    "s_waitcnt vmcnt(1)\n"
    "s_lshl_b32 s65, %[wg], 3\n"
    "s_add_u32 s65, s65, s10\n"
    "s_lshl_b32 s65, s65, 3\n"
    "v_mov_b64 v[168:169], s[12:13]\n"
    "s_mov_b64 exec, 1\n"
    "buffer_store_dwordx2 v[168:169], off, s[48:51], s65 sc1\n"
    "s_mov_b64 exec, -1\n"
    "s_waitcnt lgkmcnt(0)\n"
    "s_barrier\n"
    "v_mfma_f32_16x16x32_bf16 v[216:219], v[16:19], v[16:19], 0\n"
    "ds_read_b128 v[184:187], v52 offset:2048\n"
    "v_mul_f32 v188, v248, v200\n"
    "v_mfma_f32_16x16x32_bf16 v[216:219], v[20:23], v[20:23], v[216:219]\n"
    "v_mfma_f32_16x16x32_bf16 v[220:223], v[24:27], v[24:27], 0\n"
    "v_mul_f32 v190, v252, v200\n"
    "v_mfma_f32_16x16x32_bf16 v[220:223], v[28:31], v[28:31], v[220:223]\n"
    "s_setprio 2\n"
    "v_mfma_f32_16x16x32_bf16 v[248:251], v[0:3], v[32:35], 0\n"
    "ds_read_b32 v200, v53 offset:256\n"
    "v_fmac_f32 v188, v232, v204\n"
    "v_mfma_f32_16x16x32_bf16 v[248:251], v[4:7], v[36:39], v[248:251]\n"
    "v_mfma_f32_16x16x32_bf16 v[252:255], v[8:11], v[40:43], 0\n"
    "v_fmac_f32 v190, v236, v204\n"
    "v_mfma_f32_16x16x32_bf16 v[252:255], v[12:15], v[44:47], v[252:255]\n"
    "v_mfma_f32_16x16x32_bf16 v[232:235], v[16:19], v[32:35], 0\n"
    "ds_read_b128 v[204:207], v52 offset:3072\n"
    "s_waitcnt lgkmcnt(2)\n"
    "v_fmac_f32 v188, v216, v184\n"
    "v_mul_f32 v189, v217, v185\n"
    "v_mfma_f32_16x16x32_bf16 v[232:235], v[20:23], v[36:39], v[232:235]\n"
    "v_fmac_f32 v188, v218, v186\n"
    "v_fmac_f32 v189, v219, v187\n"
    "v_mfma_f32_16x16x32_bf16 v[236:239], v[24:27], v[40:43], 0\n"
    "v_fmac_f32 v190, v220, v184\n"
    "v_mul_f32 v191, v221, v185\n"
    "v_mfma_f32_16x16x32_bf16 v[236:239], v[28:31], v[44:47], v[236:239]\n"
    "v_fmac_f32 v190, v222, v186\n"
    "v_fmac_f32 v191, v223, v187\n"
    "v_mfma_f32_16x16x32_bf16 v[216:219], v[0:3], v[64:67], 0\n"
    "ds_read_b32 v184, v53 offset:512\n"
    "s_waitcnt lgkmcnt(2)\n"
    "v_fmac_f32 v188, v248, v200\n"
    "v_mfma_f32_16x16x32_bf16 v[216:219], v[4:7], v[68:71], v[216:219]\n"
    "v_mfma_f32_16x16x32_bf16 v[220:223], v[8:11], v[72:75], 0\n"
    "v_fmac_f32 v190, v252, v200\n"
    "v_mfma_f32_16x16x32_bf16 v[220:223], v[12:15], v[76:79], v[220:223]\n"
    "v_mfma_f32_16x16x32_bf16 v[248:251], v[16:19], v[64:67], 0\n"
    "ds_read_b128 v[200:203], v52 offset:5120\n"
    "s_waitcnt lgkmcnt(2)\n"
    "v_fmac_f32 v188, v232, v204\n"
    "v_fmac_f32 v189, v233, v205\n"
    "v_mfma_f32_16x16x32_bf16 v[248:251], v[20:23], v[68:71], v[248:251]\n"
    "v_fmac_f32 v188, v234, v206\n"
    "v_fmac_f32 v189, v235, v207\n"
    "v_mfma_f32_16x16x32_bf16 v[252:255], v[24:27], v[72:75], 0\n"
    "v_fmac_f32 v190, v236, v204\n"
    "v_fmac_f32 v191, v237, v205\n"
    "v_mfma_f32_16x16x32_bf16 v[252:255], v[28:31], v[76:79], v[252:255]\n"
    "v_fmac_f32 v190, v238, v206\n"
    "v_fmac_f32 v191, v239, v207\n"
    "v_mfma_f32_16x16x32_bf16 v[232:235], v[0:3], v[80:83], 0\n"
    "ds_read_b32 v204, v53 offset:768\n"
    "s_waitcnt lgkmcnt(2)\n"
    "v_fmac_f32 v188, v216, v184\n"
    "v_mfma_f32_16x16x32_bf16 v[232:235], v[4:7], v[84:87], v[232:235]\n"
    "v_mfma_f32_16x16x32_bf16 v[236:239], v[8:11], v[88:91], 0\n"
    "v_fmac_f32 v190, v220, v184\n"
    "v_mfma_f32_16x16x32_bf16 v[236:239], v[12:15], v[92:95], v[236:239]\n"
    "v_mfma_f32_16x16x32_bf16 v[216:219], v[16:19], v[80:83], 0\n"
    "ds_read_b128 v[184:187], v52 offset:8320\n"
    "s_waitcnt lgkmcnt(2)\n"
    "v_fmac_f32 v188, v248, v200\n"
    "v_fmac_f32 v189, v249, v201\n"
    "v_mfma_f32_16x16x32_bf16 v[216:219], v[20:23], v[84:87], v[216:219]\n"
    "v_fmac_f32 v188, v250, v202\n"
    "v_fmac_f32 v189, v251, v203\n"
    "v_mfma_f32_16x16x32_bf16 v[220:223], v[24:27], v[88:91], 0\n"
    "v_fmac_f32 v190, v252, v200\n"
    "v_fmac_f32 v191, v253, v201\n"
    "v_mfma_f32_16x16x32_bf16 v[220:223], v[28:31], v[92:95], v[220:223]\n"
    "v_fmac_f32 v190, v254, v202\n"
    "v_fmac_f32 v191, v255, v203\n"
    "v_mfma_f32_16x16x32_bf16 v[248:251], v[0:3], v[96:99], 0\n"
    "ds_read_b32 v200, v53 offset:1024\n"
    "s_waitcnt lgkmcnt(2)\n"
    "v_fmac_f32 v188, v232, v204\n"
    "v_mfma_f32_16x16x32_bf16 v[248:251], v[4:7], v[100:103], v[248:251]\n"
    "v_mfma_f32_16x16x32_bf16 v[252:255], v[8:11], v[104:107], 0\n"
    "v_fmac_f32 v190, v236, v204\n"
    "v_mfma_f32_16x16x32_bf16 v[252:255], v[12:15], v[108:111], v[252:255]\n"
    "v_mfma_f32_16x16x32_bf16 v[232:235], v[16:19], v[96:99], 0\n"
    "ds_read_b128 v[204:207], v52 offset:12416\n"
    "s_waitcnt lgkmcnt(2)\n"
    "v_fmac_f32 v188, v216, v184\n"
    "v_fmac_f32 v189, v217, v185\n"
    "v_mfma_f32_16x16x32_bf16 v[232:235], v[20:23], v[100:103], v[232:235]\n"
    "v_fmac_f32 v188, v218, v186\n"
    "v_fmac_f32 v189, v219, v187\n"
    "v_mfma_f32_16x16x32_bf16 v[236:239], v[24:27], v[104:107], 0\n"
    "v_fmac_f32 v190, v220, v184\n"
    "v_fmac_f32 v191, v221, v185\n"
    "v_mfma_f32_16x16x32_bf16 v[236:239], v[28:31], v[108:111], v[236:239]\n"
    "v_fmac_f32 v190, v222, v186\n"
    "v_fmac_f32 v191, v223, v187\n"
    "v_mfma_f32_16x16x32_bf16 v[216:219], v[0:3], v[112:115], 0\n"
    "ds_read_b32 v184, v53 offset:1280\n"
    "s_waitcnt lgkmcnt(2)\n"
    "v_fmac_f32 v188, v248, v200\n"
    "v_mfma_f32_16x16x32_bf16 v[216:219], v[4:7], v[116:119], v[216:219]\n"
    "v_mfma_f32_16x16x32_bf16 v[220:223], v[8:11], v[120:123], 0\n"
    "v_fmac_f32 v190, v252, v200\n"
    "v_mfma_f32_16x16x32_bf16 v[220:223], v[12:15], v[124:127], v[220:223]\n"
    "v_mfma_f32_16x16x32_bf16 v[248:251], v[16:19], v[112:115], 0\n"
    "ds_read_b128 v[200:203], v52 offset:17664\n"
    "s_waitcnt lgkmcnt(2)\n"
    "v_fmac_f32 v188, v232, v204\n"
    "v_fmac_f32 v189, v233, v205\n"
    "v_mfma_f32_16x16x32_bf16 v[248:251], v[20:23], v[116:119], v[248:251]\n"
    "v_fmac_f32 v188, v234, v206\n"
    "v_fmac_f32 v189, v235, v207\n"
    "v_mfma_f32_16x16x32_bf16 v[252:255], v[24:27], v[120:123], 0\n"
    "v_fmac_f32 v190, v236, v204\n"
    "v_fmac_f32 v191, v237, v205\n"
    "v_mfma_f32_16x16x32_bf16 v[252:255], v[28:31], v[124:127], v[252:255]\n"
    "v_fmac_f32 v190, v238, v206\n"
    "v_fmac_f32 v191, v239, v207\n"
    "v_mfma_f32_16x16x32_bf16 v[232:235], v[32:35], v[32:35], 0\n"
    "ds_read_b128 v[204:207], v52 offset:4096\n"
    "s_waitcnt lgkmcnt(2)\n"
    "v_fmac_f32 v188, v216, v184\n"
    "v_mfma_f32_16x16x32_bf16 v[232:235], v[36:39], v[36:39], v[232:235]\n"
    "v_mfma_f32_16x16x32_bf16 v[236:239], v[40:43], v[40:43], 0\n"
    "v_fmac_f32 v190, v220, v184\n"
    "v_mfma_f32_16x16x32_bf16 v[236:239], v[44:47], v[44:47], v[236:239]\n"
    "v_mfma_f32_16x16x32_bf16 v[216:219], v[32:35], v[64:67], 0\n"
    "ds_read_b128 v[184:187], v52 offset:6144\n"
    "s_waitcnt lgkmcnt(2)\n"
    "v_fmac_f32 v188, v248, v200\n"
    "v_fmac_f32 v189, v249, v201\n"
    "v_mfma_f32_16x16x32_bf16 v[216:219], v[36:39], v[68:71], v[216:219]\n"
    "v_fmac_f32 v188, v250, v202\n"
    "v_fmac_f32 v189, v251, v203\n"
    "v_mfma_f32_16x16x32_bf16 v[220:223], v[40:43], v[72:75], 0\n"
    "v_fmac_f32 v190, v252, v200\n"
    "v_fmac_f32 v191, v253, v201\n"
    "v_mfma_f32_16x16x32_bf16 v[220:223], v[44:47], v[76:79], v[220:223]\n"
    "v_fmac_f32 v190, v254, v202\n"
    "v_fmac_f32 v191, v255, v203\n"
    "v_mfma_f32_16x16x32_bf16 v[248:251], v[32:35], v[80:83], 0\n"
    "ds_read_b128 v[200:203], v52 offset:9344\n"
    "s_waitcnt lgkmcnt(2)\n"
    "v_fmac_f32 v188, v232, v204\n"
    "v_fmac_f32 v189, v233, v205\n"
    "v_mfma_f32_16x16x32_bf16 v[248:251], v[36:39], v[84:87], v[248:251]\n"
    "v_fmac_f32 v188, v234, v206\n"
    "v_fmac_f32 v189, v235, v207\n"
    "v_mfma_f32_16x16x32_bf16 v[252:255], v[40:43], v[88:91], 0\n"
    "v_fmac_f32 v190, v236, v204\n"
    "v_fmac_f32 v191, v237, v205\n"
    "v_mfma_f32_16x16x32_bf16 v[252:255], v[44:47], v[92:95], v[252:255]\n"
    "v_fmac_f32 v190, v238, v206\n"
    "v_fmac_f32 v191, v239, v207\n"
    "v_mfma_f32_16x16x32_bf16 v[232:235], v[32:35], v[96:99], 0\n"
    "ds_read_b128 v[204:207], v52 offset:13440\n"
    "s_waitcnt lgkmcnt(2)\n"
    "v_fmac_f32 v188, v216, v184\n"
    "v_fmac_f32 v189, v217, v185\n"
    "v_mfma_f32_16x16x32_bf16 v[232:235], v[36:39], v[100:103], v[232:235]\n"
    "v_fmac_f32 v188, v218, v186\n"
    "v_fmac_f32 v189, v219, v187\n"
    "v_mfma_f32_16x16x32_bf16 v[236:239], v[40:43], v[104:107], 0\n"
    "v_fmac_f32 v190, v220, v184\n"
    "v_fmac_f32 v191, v221, v185\n"
    "v_mfma_f32_16x16x32_bf16 v[236:239], v[44:47], v[108:111], v[236:239]\n"
    "v_fmac_f32 v190, v222, v186\n"
    "v_fmac_f32 v191, v223, v187\n"
    "v_mfma_f32_16x16x32_bf16 v[216:219], v[32:35], v[112:115], 0\n"
    "ds_read_b128 v[184:187], v52 offset:18688\n"
    "s_waitcnt lgkmcnt(2)\n"
    "v_fmac_f32 v188, v248, v200\n"
    "v_fmac_f32 v189, v249, v201\n"
    "v_mfma_f32_16x16x32_bf16 v[216:219], v[36:39], v[116:119], v[216:219]\n"
    "v_fmac_f32 v188, v250, v202\n"
    "v_fmac_f32 v189, v251, v203\n"
    "v_mfma_f32_16x16x32_bf16 v[220:223], v[40:43], v[120:123], 0\n"
    "v_fmac_f32 v190, v252, v200\n"
    "v_fmac_f32 v191, v253, v201\n"
    "v_mfma_f32_16x16x32_bf16 v[220:223], v[44:47], v[124:127], v[220:223]\n"
    "v_fmac_f32 v190, v254, v202\n"
    "v_fmac_f32 v191, v255, v203\n"
    "v_mfma_f32_16x16x32_bf16 v[248:251], v[64:67], v[64:67], 0\n"
    "ds_read_b128 v[200:203], v52 offset:7168\n"
    "s_waitcnt lgkmcnt(2)\n"
    "v_fmac_f32 v188, v232, v204\n"
    "v_fmac_f32 v189, v233, v205\n"
    "v_mfma_f32_16x16x32_bf16 v[248:251], v[68:71], v[68:71], v[248:251]\n"
    "v_fmac_f32 v188, v234, v206\n"
    "v_fmac_f32 v189, v235, v207\n"
    "v_mfma_f32_16x16x32_bf16 v[252:255], v[72:75], v[72:75], 0\n"
    "v_fmac_f32 v190, v236, v204\n"
    "v_fmac_f32 v191, v237, v205\n"
    "v_mfma_f32_16x16x32_bf16 v[252:255], v[76:79], v[76:79], v[252:255]\n"
    "v_fmac_f32 v190, v238, v206\n"
    "v_fmac_f32 v191, v239, v207\n"
    "v_mfma_f32_16x16x32_bf16 v[232:235], v[64:67], v[80:83], 0\n"
    "ds_read_b128 v[204:207], v52 offset:10368\n"
    "s_waitcnt lgkmcnt(2)\n"
    "v_fmac_f32 v188, v216, v184\n"
    "v_fmac_f32 v189, v217, v185\n"
    "v_mfma_f32_16x16x32_bf16 v[232:235], v[68:71], v[84:87], v[232:235]\n"
    "v_fmac_f32 v188, v218, v186\n"
    "v_fmac_f32 v189, v219, v187\n"
    "v_mfma_f32_16x16x32_bf16 v[236:239], v[72:75], v[88:91], 0\n"
    "v_fmac_f32 v190, v220, v184\n"
    "v_fmac_f32 v191, v221, v185\n"
    "v_mfma_f32_16x16x32_bf16 v[236:239], v[76:79], v[92:95], v[236:239]\n"
    "v_fmac_f32 v190, v222, v186\n"
    "v_fmac_f32 v191, v223, v187\n"
    "v_mfma_f32_16x16x32_bf16 v[216:219], v[64:67], v[96:99], 0\n"
    "ds_read_b128 v[184:187], v52 offset:14464\n"
    "s_waitcnt lgkmcnt(2)\n"
    "v_fmac_f32 v188, v248, v200\n"
    "v_fmac_f32 v189, v249, v201\n"
    "v_mfma_f32_16x16x32_bf16 v[216:219], v[68:71], v[100:103], v[216:219]\n"
    "v_fmac_f32 v188, v250, v202\n"
    "v_fmac_f32 v189, v251, v203\n"
    "v_mfma_f32_16x16x32_bf16 v[220:223], v[72:75], v[104:107], 0\n"
    "v_fmac_f32 v190, v252, v200\n"
    "v_fmac_f32 v191, v253, v201\n"
    "v_mfma_f32_16x16x32_bf16 v[220:223], v[76:79], v[108:111], v[220:223]\n"
    "v_fmac_f32 v190, v254, v202\n"
    "v_fmac_f32 v191, v255, v203\n"
    "v_mfma_f32_16x16x32_bf16 v[248:251], v[64:67], v[112:115], 0\n"
    "ds_read_b128 v[200:203], v52 offset:19712\n"
    "s_waitcnt lgkmcnt(2)\n"
    "v_fmac_f32 v188, v232, v204\n"
    "v_fmac_f32 v189, v233, v205\n"
    "v_mfma_f32_16x16x32_bf16 v[248:251], v[68:71], v[116:119], v[248:251]\n"
    "v_fmac_f32 v188, v234, v206\n"
    "v_fmac_f32 v189, v235, v207\n"
    "v_mfma_f32_16x16x32_bf16 v[252:255], v[72:75], v[120:123], 0\n"
    "v_fmac_f32 v190, v236, v204\n"
    "v_fmac_f32 v191, v237, v205\n"
    "v_mfma_f32_16x16x32_bf16 v[252:255], v[76:79], v[124:127], v[252:255]\n"
    "v_fmac_f32 v190, v238, v206\n"
    "v_fmac_f32 v191, v239, v207\n"
    "v_mfma_f32_16x16x32_bf16 v[232:235], v[80:83], v[80:83], 0\n"
    "ds_read_b128 v[204:207], v52 offset:11392\n"
    "s_waitcnt lgkmcnt(2)\n"
    "v_fmac_f32 v188, v216, v184\n"
    "v_fmac_f32 v189, v217, v185\n"
    "v_mfma_f32_16x16x32_bf16 v[232:235], v[84:87], v[84:87], v[232:235]\n"
    "v_fmac_f32 v188, v218, v186\n"
    "v_fmac_f32 v189, v219, v187\n"
    "v_mfma_f32_16x16x32_bf16 v[236:239], v[88:91], v[88:91], 0\n"
    "v_fmac_f32 v190, v220, v184\n"
    "v_fmac_f32 v191, v221, v185\n"
    "v_mfma_f32_16x16x32_bf16 v[236:239], v[92:95], v[92:95], v[236:239]\n"
    "v_fmac_f32 v190, v222, v186\n"
    "v_fmac_f32 v191, v223, v187\n"
    "v_mfma_f32_16x16x32_bf16 v[216:219], v[80:83], v[96:99], 0\n"
    "ds_read_b128 v[184:187], v52 offset:15488\n"
    "s_waitcnt lgkmcnt(2)\n"
    "v_fmac_f32 v188, v248, v200\n"
    "v_fmac_f32 v189, v249, v201\n"
    "s_add_u32 m0, s54, 0\n"
    "s_add_u32 s16, s56, 0\n"
    "buffer_load_dwordx4 v48, s[36:39], s16 offen lds\n"
    "v_mfma_f32_16x16x32_bf16 v[216:219], v[84:87], v[100:103], v[216:219]\n"
    "v_fmac_f32 v188, v250, v202\n"
    "v_fmac_f32 v189, v251, v203\n"
    "v_mfma_f32_16x16x32_bf16 v[220:223], v[88:91], v[104:107], 0\n"
    "v_fmac_f32 v190, v252, v200\n"
    "v_fmac_f32 v191, v253, v201\n"
    "s_add_u32 m0, s54, 1024\n"
    "s_nop 0\n"
    "buffer_load_dwordx4 v49, s[36:39], s16 offen lds\n"
    "v_mfma_f32_16x16x32_bf16 v[220:223], v[92:95], v[108:111], v[220:223]\n"
    "v_fmac_f32 v190, v254, v202\n"
    "v_fmac_f32 v191, v255, v203\n"
    "v_mfma_f32_16x16x32_bf16 v[248:251], v[80:83], v[112:115], 0\n"
    "ds_read_b128 v[200:203], v52 offset:20736\n"
    "s_waitcnt lgkmcnt(2)\n"
    "v_fmac_f32 v188, v232, v204\n"
    "v_fmac_f32 v189, v233, v205\n"
    "s_add_u32 m0, s54, 2048\n"
    "s_add_u32 s16, s56, 4096\n"
    "buffer_load_dwordx4 v48, s[36:39], s16 offen lds\n"
    "ds_read_b64_tr_b16 v[0:1], v56\n"
    "ds_read_b64_tr_b16 v[2:3], v56 offset:64\n"
    "v_mfma_f32_16x16x32_bf16 v[248:251], v[84:87], v[116:119], v[248:251]\n"
    "v_fmac_f32 v188, v234, v206\n"
    "v_fmac_f32 v189, v235, v207\n"
    "ds_read_b64_tr_b16 v[4:5], v56 offset:512\n"
    "ds_read_b64_tr_b16 v[6:7], v56 offset:576\n"
    "v_mfma_f32_16x16x32_bf16 v[252:255], v[88:91], v[120:123], 0\n"
    "v_fmac_f32 v190, v236, v204\n"
    "v_fmac_f32 v191, v237, v205\n"
    "s_add_u32 m0, s54, 3072\n"
    "s_nop 0\n"
    "buffer_load_dwordx4 v49, s[36:39], s16 offen lds\n"
    "ds_read_b64_tr_b16 v[8:9], v56 offset:1024\n"
    "ds_read_b64_tr_b16 v[10:11], v56 offset:1088\n"
    "v_mfma_f32_16x16x32_bf16 v[252:255], v[92:95], v[124:127], v[252:255]\n"
    "v_fmac_f32 v190, v238, v206\n"
    "v_fmac_f32 v191, v239, v207\n"
    "ds_read_b64_tr_b16 v[12:13], v56 offset:1536\n"
    "ds_read_b64_tr_b16 v[14:15], v56 offset:1600\n"
    "v_mfma_f32_16x16x32_bf16 v[232:235], v[96:99], v[96:99], 0\n"
    "ds_read_b128 v[204:207], v52 offset:16640\n"
    "s_waitcnt lgkmcnt(10)\n"
    "v_fmac_f32 v188, v216, v184\n"
    "v_fmac_f32 v189, v217, v185\n"
    "s_add_u32 m0, s54, 4096\n"
    "s_add_u32 s16, s56, 8192\n"
    "buffer_load_dwordx4 v48, s[36:39], s16 offen lds\n"
    "ds_read_b64_tr_b16 v[16:17], v56 offset:8\n"
    "ds_read_b64_tr_b16 v[18:19], v56 offset:72\n"
    "v_mfma_f32_16x16x32_bf16 v[232:235], v[100:103], v[100:103], v[232:235]\n"
    "v_fmac_f32 v188, v218, v186\n"
    "v_fmac_f32 v189, v219, v187\n"
    "ds_read_b64_tr_b16 v[20:21], v56 offset:520\n"
    "ds_read_b64_tr_b16 v[22:23], v56 offset:584\n"
    "v_mfma_f32_16x16x32_bf16 v[236:239], v[104:107], v[104:107], 0\n"
    "v_fmac_f32 v190, v220, v184\n"
    "v_fmac_f32 v191, v221, v185\n"
    "s_add_u32 m0, s54, 5120\n"
    "s_nop 0\n"
    "buffer_load_dwordx4 v49, s[36:39], s16 offen lds\n"
    "ds_read_b64_tr_b16 v[24:25], v56 offset:1032\n"
    "ds_read_b64_tr_b16 v[26:27], v56 offset:1096\n"
    "v_mfma_f32_16x16x32_bf16 v[236:239], v[108:111], v[108:111], v[236:239]\n"
    "v_fmac_f32 v190, v222, v186\n"
    "v_fmac_f32 v191, v223, v187\n"
    "ds_read_b64_tr_b16 v[28:29], v56 offset:1544\n"
    "ds_read_b64_tr_b16 v[30:31], v56 offset:1608\n"
    "v_mfma_f32_16x16x32_bf16 v[216:219], v[96:99], v[112:115], 0\n"
    "ds_read_b128 v[184:187], v52 offset:21760\n"
    "s_waitcnt lgkmcnt(15)\n"
    "v_fmac_f32 v188, v248, v200\n"
    "v_fmac_f32 v189, v249, v201\n"
    "s_add_u32 m0, s54, 6144\n"
    "s_add_u32 s16, s56, 12288\n"
    "buffer_load_dwordx4 v48, s[36:39], s16 offen lds\n"
    "v_mfma_f32_16x16x32_bf16 v[216:219], v[100:103], v[116:119], v[216:219]\n"
    "v_fmac_f32 v188, v250, v202\n"
    "v_fmac_f32 v189, v251, v203\n"
    "v_mfma_f32_16x16x32_bf16 v[220:223], v[104:107], v[120:123], 0\n"
    "v_fmac_f32 v190, v252, v200\n"
    "v_fmac_f32 v191, v253, v201\n"
    "s_add_u32 m0, s54, 7168\n"
    "s_nop 0\n"
    "buffer_load_dwordx4 v49, s[36:39], s16 offen lds\n"
    "v_mfma_f32_16x16x32_bf16 v[220:223], v[108:111], v[124:127], v[220:223]\n"
    "v_fmac_f32 v190, v254, v202\n"
    "v_fmac_f32 v191, v255, v203\n"
    "v_mov_b32 v156, -1\n"
    "v_mov_b32 v157, -1\n"
    "v_mov_b32 v158, -1\n"
    "v_mov_b32 v159, -1\n"
    "buffer_store_dwordx4 v[156:159], v57, s[48:51], s18 offen sc1\n"
    "v_mfma_f32_16x16x32_bf16 v[248:251], v[112:115], v[112:115], 0\n"
    "ds_read_b128 v[200:203], v52 offset:22784\n"
    "s_waitcnt lgkmcnt(10)\n"
    "v_fmac_f32 v188, v232, v204\n"
    "v_fmac_f32 v189, v233, v205\n"
    "v_mfma_f32_16x16x32_bf16 v[248:251], v[116:119], v[116:119], v[248:251]\n"
    "v_fmac_f32 v188, v234, v206\n"
    "v_fmac_f32 v189, v235, v207\n"
    "v_mfma_f32_16x16x32_bf16 v[252:255], v[120:123], v[120:123], 0\n"
    "v_fmac_f32 v190, v236, v204\n"
    "v_fmac_f32 v191, v237, v205\n"
    "v_mfma_f32_16x16x32_bf16 v[252:255], v[124:127], v[124:127], v[252:255]\n"
    "v_fmac_f32 v190, v238, v206\n"
    "v_fmac_f32 v191, v239, v207\n"
    "s_waitcnt vmcnt(7)\n"
    "ds_read_b128 v[136:139], v50\n"
    "ds_read_b128 v[140:143], v51\n"
    "s_waitcnt lgkmcnt(3)\n"
    "v_fmac_f32 v188, v216, v184\n"
    "v_fmac_f32 v189, v217, v185\n"
    "v_fmac_f32 v188, v218, v186\n"
    "v_fmac_f32 v189, v219, v187\n"
    "v_fmac_f32 v190, v220, v184\n"
    "v_fmac_f32 v191, v221, v185\n"
    "v_fmac_f32 v190, v222, v186\n"
    "v_fmac_f32 v191, v223, v187\n"
    "v_mov_b32 v120, v152\n"
    "v_mov_b32 v121, v153\n"
    "v_mov_b32 v122, v154\n"
    "v_mov_b32 v123, v155\n"
    "s_waitcnt lgkmcnt(2)\n"
    "v_fmac_f32 v188, v248, v200\n"
    "v_fmac_f32 v189, v249, v201\n"
    "v_fmac_f32 v188, v250, v202\n"
    "v_fmac_f32 v189, v251, v203\n"
    "v_fmac_f32 v190, v252, v200\n"
    "v_fmac_f32 v191, v253, v201\n"
    "v_fmac_f32 v190, v254, v202\n"
    "v_fmac_f32 v191, v255, v203\n"
    "s_lshl_b32 s67, s11, 3\n"
    "s_add_u32 s67, s67, s10\n"
    "s_lshl_b32 s67, s67, 9\n"
    "buffer_load_dwordx2 v[48:49], v55, s[48:51], s67 offen sc1\n"
    "s_add_u32 s57, s57, 1048576\n"
    "s_mul_i32 s56, s11, 56\n"
    "s_add_u32 s56, s56, s10\n"
    "s_lshl_b32 s56, s56, 12\n"
    "s_add_u32 s56, s56, 262144\n"
    "s_mov_b32 m0, s54\n"
    "s_waitcnt vmcnt(6)\n"
    "ds_read_b128 v[152:155], v50 offset:2048\n"
    "ds_read_b128 v[156:159], v51 offset:2048\n"
    "v_mfma_f32_16x16x32_bf16 v[32:35], v[128:131], v[0:3], 0\n"
    "v_add_f32 v188, v188, v189\n"
    "v_add_f32 v190, v190, v191\n"
    "v_mfma_f32_16x16x32_bf16 v[32:35], v[132:135], v[4:7], v[32:35]\n"
    "v_add_u32 v124, 0x10000, v50\n"
    "v_add_u32 v125, 0x10000, v51\n"
    "s_waitcnt lgkmcnt(2)\n"
    "v_mfma_f32_16x16x32_bf16 v[32:35], v[136:139], v[8:11], v[32:35]\n"
    "v_mov_b32 v126, v54\n"
    "v_permlane32_swap_b32 v188, v190\n"
    "v_mfma_f32_16x16x32_bf16 v[32:35], v[140:143], v[12:15], v[32:35]\n"
    "v_add_f32 v188, v188, v190\n"
    "v_mov_b32 v189, v188\n"
    "v_mfma_f32_16x16x32_bf16 v[36:39], v[128:131], v[16:19], 0\n"
    "s_nop 0\n"
    "v_permlane16_swap_b32 v188, v189\n"
    "v_add_f32 v188, v188, v189\n"
    "v_mfma_f32_16x16x32_bf16 v[36:39], v[132:135], v[20:23], v[36:39]\n"
    "s_nop 0\n"
    "v_add_f32_dpp v188, v188, v188 row_ror:8 row_mask:0xf bank_mask:0xf\n"
    "s_nop 1\n"
    "v_add_f32_dpp v188, v188, v188 row_ror:4 row_mask:0xf bank_mask:0xf\n"
    "v_mfma_f32_16x16x32_bf16 v[36:39], v[136:139], v[24:27], v[36:39]\n"
    "s_nop 0\n"
    "v_add_f32_dpp v188, v188, v188 row_ror:2 row_mask:0xf bank_mask:0xf\n"
    "s_nop 1\n"
    "v_add_f32_dpp v188, v188, v188 row_ror:1 row_mask:0xf bank_mask:0xf\n"
    "v_mfma_f32_16x16x32_bf16 v[36:39], v[140:143], v[28:31], v[36:39]\n"
    "v_lshl_add_u32 v189, s10, 5, v55\n"
    "v_lshrrev_b32 v189, 3, v189\n"
    "s_barrier\n"
    "s_add_u32 s65, s53, 8320\n"
    "v_add_u32 v127, s65, v57\n"
    "ds_write2_b64 v127, v[120:121], v[122:123] offset1:1\n"
    "s_waitcnt vmcnt(4)\n"
    "ds_read_b128 v[168:171], v50 offset:4096\n"
    "ds_read_b128 v[172:175], v51 offset:4096\n"
    "v_mfma_f32_16x16x32_bf16 v[40:43], v[144:147], v[0:3], 0\n"
    "v_add_u32 v189, 163264, v189\n"
    "v_mfma_f32_16x16x32_bf16 v[40:43], v[148:151], v[4:7], v[40:43]\n"
    "s_mov_b32 exec_lo, 1\n"
    "s_mov_b32 exec_hi, 1\n"
    "ds_write_b32 v189, v188\n"
    "s_mov_b64 exec, -1\n"
    "s_waitcnt lgkmcnt(4)\n"
    "v_mfma_f32_16x16x32_bf16 v[40:43], v[152:155], v[8:11], v[40:43]\n"
    "v_mov_b64 v[64:65], 0\n"
    "v_mfma_f32_16x16x32_bf16 v[40:43], v[156:159], v[12:15], v[40:43]\n"
    "v_mfma_f32_16x16x32_bf16 v[44:47], v[144:147], v[16:19], 0\n"
    "v_cvt_pk_bf16_f32 v36, v32, v36\n"
    "v_mov_b64 v[66:67], 0\n"
    "v_mfma_f32_16x16x32_bf16 v[44:47], v[148:151], v[20:23], v[44:47]\n"
    "v_cvt_pk_bf16_f32 v37, v33, v37\n"
    "v_mfma_f32_16x16x32_bf16 v[44:47], v[152:155], v[24:27], v[44:47]\n"
    "v_cvt_pk_bf16_f32 v38, v34, v38\n"
    "v_mov_b64 v[68:69], 0\n"
    "v_mfma_f32_16x16x32_bf16 v[44:47], v[156:159], v[28:31], v[44:47]\n"
    "v_cvt_pk_bf16_f32 v39, v35, v39\n"
    "s_waitcnt vmcnt(2)\n"
    "ds_read_b128 v[184:187], v50 offset:6144\n"
    "ds_read_b128 v[188:191], v51 offset:6144\n"
    "v_mfma_f32_16x16x32_bf16 v[32:35], v[160:163], v[0:3], 0\n"
    "ds_write_addtid_b32 v36 offset:0\n"
    "v_mov_b64 v[70:71], 0\n"
    "v_mfma_f32_16x16x32_bf16 v[32:35], v[164:167], v[4:7], v[32:35]\n"
    "ds_write_addtid_b32 v37 offset:256\n"
    "s_waitcnt lgkmcnt(5)\n"
    "v_mfma_f32_16x16x32_bf16 v[32:35], v[168:171], v[8:11], v[32:35]\n"
    "ds_write_addtid_b32 v38 offset:512\n"
    "v_mov_b64 v[72:73], 0\n"
    "v_mfma_f32_16x16x32_bf16 v[32:35], v[172:175], v[12:15], v[32:35]\n"
    "ds_write_addtid_b32 v39 offset:768\n"
    "v_mfma_f32_16x16x32_bf16 v[36:39], v[160:163], v[16:19], 0\n"
    "v_cvt_pk_bf16_f32 v44, v40, v44\n"
    "v_mov_b64 v[74:75], 0\n"
    "v_mfma_f32_16x16x32_bf16 v[36:39], v[164:167], v[20:23], v[36:39]\n"
    "v_cvt_pk_bf16_f32 v45, v41, v45\n"
    "v_mfma_f32_16x16x32_bf16 v[36:39], v[168:171], v[24:27], v[36:39]\n"
    "v_cvt_pk_bf16_f32 v46, v42, v46\n"
    "v_mov_b64 v[76:77], 0\n"
    "v_mfma_f32_16x16x32_bf16 v[36:39], v[172:175], v[28:31], v[36:39]\n"
    "v_cvt_pk_bf16_f32 v47, v43, v47\n"
    "ds_read_b128 v[200:203], v124\n"
    "ds_read_b128 v[204:207], v125\n"
    "v_mfma_f32_16x16x32_bf16 v[40:43], v[176:179], v[0:3], 0\n"
    "ds_write_addtid_b32 v44 offset:1024\n"
    "v_mov_b64 v[78:79], 0\n"
    "v_mfma_f32_16x16x32_bf16 v[40:43], v[180:183], v[4:7], v[40:43]\n"
    "ds_write_addtid_b32 v45 offset:1280\n"
    "s_waitcnt lgkmcnt(8)\n"
    "v_mfma_f32_16x16x32_bf16 v[40:43], v[184:187], v[8:11], v[40:43]\n"
    "ds_write_addtid_b32 v46 offset:1536\n"
    "v_mov_b64 v[80:81], 0\n"
    "v_mfma_f32_16x16x32_bf16 v[40:43], v[188:191], v[12:15], v[40:43]\n"
    "ds_write_addtid_b32 v47 offset:1792\n"
    "v_mfma_f32_16x16x32_bf16 v[44:47], v[176:179], v[16:19], 0\n"
    "v_cvt_pk_bf16_f32 v36, v32, v36\n"
    "v_mov_b64 v[82:83], 0\n"
    "v_mfma_f32_16x16x32_bf16 v[44:47], v[180:183], v[20:23], v[44:47]\n"
    "v_cvt_pk_bf16_f32 v37, v33, v37\n"
    "v_mfma_f32_16x16x32_bf16 v[44:47], v[184:187], v[24:27], v[44:47]\n"
    "v_cvt_pk_bf16_f32 v38, v34, v38\n"
    "v_mov_b64 v[84:85], 0\n"
    "v_mfma_f32_16x16x32_bf16 v[44:47], v[188:191], v[28:31], v[44:47]\n"
    "v_cvt_pk_bf16_f32 v39, v35, v39\n"
    "ds_read_b128 v[216:219], v124 offset:2048\n"
    "ds_read_b128 v[220:223], v125 offset:2048\n"
    "s_waitcnt vmcnt(0)\n"
    "s_mov_b32 s66, 65536\n"
    "L_p1Ba_%=:\n"
    "v_cmp_ne_u64_e64 s[14:15], v[48:49], s[12:13]\n"
    "s_nop 3\n"
    "s_or_b32 s14, s14, s15\n"
    "s_and_b32 s14, s14, 0xf0f0f0f0\n"
    "s_cbranch_scc0 L_p1Bb_%=\n"
    "s_sleep 2\n"
    "buffer_load_dwordx2 v[48:49], v55, s[48:51], s67 offen sc1\n"
    "s_waitcnt vmcnt(0)\n"
    "s_sub_u32 s66, s66, 1\n"
    "s_cmp_lg_u32 s66, 0\n"
    "s_cbranch_scc1 L_p1Ba_%=\n"
    "L_p1Bb_%=:\n"
    "buffer_load_dwordx4 v[48:51], v57, s[48:51], s56 offen sc1\n"
    "buffer_load_dwordx4 v[52:55], v57, s[48:51], s56 offen offset:1024 sc1\n"
    "v_mfma_f32_16x16x32_bf16 v[32:35], v[192:195], v[0:3], 0\n"
    "ds_write_addtid_b32 v36 offset:2048\n"
    "v_mov_b64 v[86:87], 0\n"
    "v_mfma_f32_16x16x32_bf16 v[32:35], v[196:199], v[4:7], v[32:35]\n"
    "ds_write_addtid_b32 v37 offset:2304\n"
    "s_waitcnt lgkmcnt(8)\n"
    "v_mfma_f32_16x16x32_bf16 v[32:35], v[200:203], v[8:11], v[32:35]\n"
    "ds_write_addtid_b32 v38 offset:2560\n"
    "v_mov_b64 v[88:89], 0\n"
    "v_mfma_f32_16x16x32_bf16 v[32:35], v[204:207], v[12:15], v[32:35]\n"
    "ds_write_addtid_b32 v39 offset:2816\n"
    "v_mfma_f32_16x16x32_bf16 v[36:39], v[192:195], v[16:19], 0\n"
    "v_cvt_pk_bf16_f32 v44, v40, v44\n"
    "v_mov_b64 v[90:91], 0\n"
    "v_mfma_f32_16x16x32_bf16 v[36:39], v[196:199], v[20:23], v[36:39]\n"
    "v_cvt_pk_bf16_f32 v45, v41, v45\n"
    "v_mfma_f32_16x16x32_bf16 v[36:39], v[200:203], v[24:27], v[36:39]\n"
    "v_cvt_pk_bf16_f32 v46, v42, v46\n"
    "v_mov_b64 v[92:93], 0\n"
    "v_mfma_f32_16x16x32_bf16 v[36:39], v[204:207], v[28:31], v[36:39]\n"
    "v_cvt_pk_bf16_f32 v47, v43, v47\n"
    "ds_read_b128 v[232:235], v124 offset:4096\n"
    "ds_read_b128 v[236:239], v125 offset:4096\n"
    "v_mfma_f32_16x16x32_bf16 v[40:43], v[208:211], v[0:3], 0\n"
    "ds_write_addtid_b32 v44 offset:3072\n"
    "v_mov_b64 v[94:95], 0\n"
    "v_mfma_f32_16x16x32_bf16 v[40:43], v[212:215], v[4:7], v[40:43]\n"
    "ds_write_addtid_b32 v45 offset:3328\n"
    "s_waitcnt lgkmcnt(8)\n"
    "v_mfma_f32_16x16x32_bf16 v[40:43], v[216:219], v[8:11], v[40:43]\n"
    "ds_write_addtid_b32 v46 offset:3584\n"
    "v_mov_b64 v[96:97], 0\n"
    "v_mfma_f32_16x16x32_bf16 v[40:43], v[220:223], v[12:15], v[40:43]\n"
    "ds_write_addtid_b32 v47 offset:3840\n"
    "v_mfma_f32_16x16x32_bf16 v[44:47], v[208:211], v[16:19], 0\n"
    "v_cvt_pk_bf16_f32 v36, v32, v36\n"
    "v_mov_b64 v[98:99], 0\n"
    "v_mfma_f32_16x16x32_bf16 v[44:47], v[212:215], v[20:23], v[44:47]\n"
    "v_cvt_pk_bf16_f32 v37, v33, v37\n"
    "v_mfma_f32_16x16x32_bf16 v[44:47], v[216:219], v[24:27], v[44:47]\n"
    "v_cvt_pk_bf16_f32 v38, v34, v38\n"
    "v_mov_b64 v[100:101], 0\n"
    "v_mfma_f32_16x16x32_bf16 v[44:47], v[220:223], v[28:31], v[44:47]\n"
    "v_cvt_pk_bf16_f32 v39, v35, v39\n"
    "s_mul_i32 s66, s10, 800\n"
    "s_add_u32 m0, s66, 156032\n"
    "s_mov_b32 exec_hi, 0x3ffff\n"
    "buffer_load_dwordx4 v126, s[4:7], s61 offen lds\n"
    "s_mov_b32 exec_hi, -1\n"
    "s_mov_b32 m0, s54\n"
    "ds_read_b128 v[248:251], v124 offset:6144\n"
    "ds_read_b128 v[252:255], v125 offset:6144\n"
    "v_mfma_f32_16x16x32_bf16 v[32:35], v[224:227], v[0:3], 0\n"
    "ds_write_addtid_b32 v36 offset:4096\n"
    "v_mov_b64 v[102:103], 0\n"
    "v_mfma_f32_16x16x32_bf16 v[32:35], v[228:231], v[4:7], v[32:35]\n"
    "ds_write_addtid_b32 v37 offset:4352\n"
    "s_waitcnt lgkmcnt(8)\n"
    "v_mfma_f32_16x16x32_bf16 v[32:35], v[232:235], v[8:11], v[32:35]\n"
    "ds_write_addtid_b32 v38 offset:4608\n"
    "v_mov_b64 v[104:105], 0\n"
    "v_mfma_f32_16x16x32_bf16 v[32:35], v[236:239], v[12:15], v[32:35]\n"
    "ds_write_addtid_b32 v39 offset:4864\n"
    "v_mfma_f32_16x16x32_bf16 v[36:39], v[224:227], v[16:19], 0\n"
    "v_cvt_pk_bf16_f32 v44, v40, v44\n"
    "v_mov_b64 v[106:107], 0\n"
    "v_mfma_f32_16x16x32_bf16 v[36:39], v[228:231], v[20:23], v[36:39]\n"
    "v_cvt_pk_bf16_f32 v45, v41, v45\n"
    "v_mfma_f32_16x16x32_bf16 v[36:39], v[232:235], v[24:27], v[36:39]\n"
    "v_cvt_pk_bf16_f32 v46, v42, v46\n"
    "v_mov_b64 v[108:109], 0\n"
    "v_mfma_f32_16x16x32_bf16 v[36:39], v[236:239], v[28:31], v[36:39]\n"
    "v_cvt_pk_bf16_f32 v47, v43, v47\n"
    "v_mfma_f32_16x16x32_bf16 v[40:43], v[240:243], v[0:3], 0\n"
    "ds_write_addtid_b32 v44 offset:5120\n"
    "v_mov_b64 v[110:111], 0\n"
    "v_mfma_f32_16x16x32_bf16 v[40:43], v[244:247], v[4:7], v[40:43]\n"
    "ds_write_addtid_b32 v45 offset:5376\n"
    "s_waitcnt lgkmcnt(6)\n"
    "v_mfma_f32_16x16x32_bf16 v[40:43], v[248:251], v[8:11], v[40:43]\n"
    "ds_write_addtid_b32 v46 offset:5632\n"
    "v_mov_b64 v[112:113], 0\n"
    "v_mfma_f32_16x16x32_bf16 v[40:43], v[252:255], v[12:15], v[40:43]\n"
    "ds_write_addtid_b32 v47 offset:5888\n"
    "v_mfma_f32_16x16x32_bf16 v[44:47], v[240:243], v[16:19], 0\n"
    "v_cvt_pk_bf16_f32 v36, v32, v36\n"
    "v_mov_b64 v[114:115], 0\n"
    "v_mfma_f32_16x16x32_bf16 v[44:47], v[244:247], v[20:23], v[44:47]\n"
    "v_cvt_pk_bf16_f32 v37, v33, v37\n"
    "v_mfma_f32_16x16x32_bf16 v[44:47], v[248:251], v[24:27], v[44:47]\n"
    "v_cvt_pk_bf16_f32 v38, v34, v38\n"
    "v_mov_b64 v[116:117], 0\n"
    "v_mfma_f32_16x16x32_bf16 v[44:47], v[252:255], v[28:31], v[44:47]\n"
    "v_cvt_pk_bf16_f32 v39, v35, v39\n"
    "buffer_load_dwordx4 v[32:35], v57, s[48:51], s56 offen offset:2048 sc1\n"
    "ds_write_addtid_b32 v36 offset:6144\n"
    "v_mov_b64 v[118:119], 0\n"
    "ds_write_addtid_b32 v37 offset:6400\n"
    "ds_write_addtid_b32 v38 offset:6656\n"
    "ds_write_addtid_b32 v39 offset:6912\n"
    "buffer_load_dwordx4 v[36:39], v57, s[48:51], s56 offen offset:3072 sc1\n"
    "v_cvt_pk_bf16_f32 v44, v40, v44\n"
    "v_cvt_pk_bf16_f32 v45, v41, v45\n"
    "v_cvt_pk_bf16_f32 v46, v42, v46\n"
    "v_cvt_pk_bf16_f32 v47, v43, v47\n"
    "ds_write_addtid_b32 v44 offset:7168\n"
    "ds_write_addtid_b32 v45 offset:7424\n"
    "ds_write_addtid_b32 v46 offset:7680\n"
    "ds_write_addtid_b32 v47 offset:7936\n"
    "v_mov_b64 v[120:121], 0\n"
    "v_mov_b64 v[122:123], 0\n"
    "v_mov_b64 v[124:125], 0\n"
    "v_mov_b64 v[126:127], 0\n"
    "s_add_u32 s57, s57, 524288\n"
    "s_add_u32 s58, s57, 524288\n"
    "s_mov_b32 s61, s53\n"
    "s_mov_b32 s62, 0\n"
    "s_mov_b32 s63, 1\n"
    "s_mov_b32 s64, -1\n"
    "s_add_u32 s31, s18, 1024\n"
    "v_add_u32 v56, 8320, v56\n"
    "s_mov_b32 s43, 0xffff\n"
    "s_mov_b32 s67, 8320\n"
    "s_setprio 0\n"
    "s_branch L_EG0_%=\n"
    "L_RA_%=:\n"
    "s_setprio 3\n"
    "ds_read_b128 v[0:3], v0\n"
    "ds_read_b128 v[8:11], v8\n"
    "ds_read_b128 v[4:7], v4\n"
    "ds_read_b128 v[12:15], v12\n"
    "ds_read_b128 v[16:19], v16\n"
    "ds_read_b128 v[24:27], v24\n"
    "ds_read_b128 v[20:23], v20\n"
    "ds_read_b128 v[28:31], v28\n"
    "ds_read_b128 v[32:35], v32\n"
    "ds_read_b128 v[40:43], v40\n"
    "ds_read_b128 v[36:39], v36\n"
    "ds_read_b128 v[44:47], v44\n"
    "ds_read_b128 v[64:67], v64\n"
    "ds_read_b128 v[72:75], v72\n"
    "s_setprio 3\n"
    "s_waitcnt lgkmcnt(13)\n"
    "v_mfma_f32_16x16x32_bf16 v[248:251], v[0:3], v[0:3], 0\n"
    "ds_read_b32 v200, v53 offset:1536\n"
    "ds_read_b128 v[68:71], v68\n"
    "s_waitcnt lgkmcnt(13)\n"
    "v_mfma_f32_16x16x32_bf16 v[248:251], v[4:7], v[4:7], v[248:251]\n"
    "v_mfma_f32_16x16x32_bf16 v[252:255], v[8:11], v[8:11], 0\n"
    "ds_read_b128 v[76:79], v76\n"
    "s_waitcnt lgkmcnt(13)\n"
    "v_mfma_f32_16x16x32_bf16 v[252:255], v[12:15], v[12:15], v[252:255]\n"
    "s_waitcnt lgkmcnt(12)\n"
    "v_mfma_f32_16x16x32_bf16 v[232:235], v[0:3], v[16:19], 0\n"
    "ds_read_b32 v204, v53\n"
    "ds_read_b128 v[80:83], v80\n"
    "s_waitcnt lgkmcnt(12)\n"
    "v_mfma_f32_16x16x32_bf16 v[232:235], v[4:7], v[20:23], v[232:235]\n"
    "v_mfma_f32_16x16x32_bf16 v[236:239], v[8:11], v[24:27], 0\n"
    "ds_read_b128 v[88:91], v88\n"
    "s_waitcnt lgkmcnt(12)\n"
    "v_mfma_f32_16x16x32_bf16 v[236:239], v[12:15], v[28:31], v[236:239]\n"
    "v_mfma_f32_16x16x32_bf16 v[216:219], v[16:19], v[16:19], 0\n"
    "ds_read_b128 v[184:187], v52 offset:2048\n"
    "s_waitcnt lgkmcnt(6)\n"
    "v_mul_f32 v188, v248, v200\n"
    "ds_read_b128 v[84:87], v84\n"
    "v_mfma_f32_16x16x32_bf16 v[216:219], v[20:23], v[20:23], v[216:219]\n"
    "v_mfma_f32_16x16x32_bf16 v[220:223], v[24:27], v[24:27], 0\n"
    "v_mul_f32 v190, v252, v200\n"
    "ds_read_b128 v[92:95], v92\n"
    "v_mfma_f32_16x16x32_bf16 v[220:223], v[28:31], v[28:31], v[220:223]\n"
    "v_mfma_f32_16x16x32_bf16 v[248:251], v[0:3], v[32:35], 0\n"
    "ds_read_b32 v200, v53 offset:256\n"
    "s_waitcnt lgkmcnt(6)\n"
    "v_fmac_f32 v188, v232, v204\n"
    "ds_read_b128 v[96:99], v96\n"
    "v_mfma_f32_16x16x32_bf16 v[248:251], v[4:7], v[36:39], v[248:251]\n"
    "v_mfma_f32_16x16x32_bf16 v[252:255], v[8:11], v[40:43], 0\n"
    "v_fmac_f32 v190, v236, v204\n"
    "ds_read_b128 v[104:107], v104\n"
    "v_mfma_f32_16x16x32_bf16 v[252:255], v[12:15], v[44:47], v[252:255]\n"
    "v_mfma_f32_16x16x32_bf16 v[232:235], v[16:19], v[32:35], 0\n"
    "ds_read_b128 v[204:207], v52 offset:3072\n"
    "s_waitcnt lgkmcnt(6)\n"
    "v_fmac_f32 v188, v216, v184\n"
    "v_mul_f32 v189, v217, v185\n"
    "ds_read_b128 v[100:103], v100\n"
    "v_mfma_f32_16x16x32_bf16 v[232:235], v[20:23], v[36:39], v[232:235]\n"
    "v_fmac_f32 v188, v218, v186\n"
    "v_fmac_f32 v189, v219, v187\n"
    "v_mfma_f32_16x16x32_bf16 v[236:239], v[24:27], v[40:43], 0\n"
    "v_fmac_f32 v190, v220, v184\n"
    "v_mul_f32 v191, v221, v185\n"
    "ds_read_b128 v[108:111], v108\n"
    "v_mfma_f32_16x16x32_bf16 v[236:239], v[28:31], v[44:47], v[236:239]\n"
    "v_fmac_f32 v190, v222, v186\n"
    "v_fmac_f32 v191, v223, v187\n"
    "v_mfma_f32_16x16x32_bf16 v[216:219], v[0:3], v[64:67], 0\n"
    "ds_read_b32 v184, v53 offset:512\n"
    "s_waitcnt lgkmcnt(6)\n"
    "v_fmac_f32 v188, v248, v200\n"
    "ds_read_b128 v[112:115], v112\n"
    "v_mfma_f32_16x16x32_bf16 v[216:219], v[4:7], v[68:71], v[216:219]\n"
    "v_mfma_f32_16x16x32_bf16 v[220:223], v[8:11], v[72:75], 0\n"
    "v_fmac_f32 v190, v252, v200\n"
    "ds_read_b128 v[120:123], v120\n"
    "v_mfma_f32_16x16x32_bf16 v[220:223], v[12:15], v[76:79], v[220:223]\n"
    "v_mfma_f32_16x16x32_bf16 v[248:251], v[16:19], v[64:67], 0\n"
    "ds_read_b128 v[200:203], v52 offset:5120\n"
    "s_waitcnt lgkmcnt(6)\n"
    "v_fmac_f32 v188, v232, v204\n"
    "v_fmac_f32 v189, v233, v205\n"
    "ds_read_b128 v[116:119], v116\n"
    "v_mfma_f32_16x16x32_bf16 v[248:251], v[20:23], v[68:71], v[248:251]\n"
    "v_fmac_f32 v188, v234, v206\n"
    "v_fmac_f32 v189, v235, v207\n"
    "v_mfma_f32_16x16x32_bf16 v[252:255], v[24:27], v[72:75], 0\n"
    "v_fmac_f32 v190, v236, v204\n"
    "v_fmac_f32 v191, v237, v205\n"
    "ds_read_b128 v[124:127], v124\n"
    "v_mfma_f32_16x16x32_bf16 v[252:255], v[28:31], v[76:79], v[252:255]\n"
    "v_fmac_f32 v190, v238, v206\n"
    "v_fmac_f32 v191, v239, v207\n"
    "v_mfma_f32_16x16x32_bf16 v[232:235], v[0:3], v[80:83], 0\n"
    "ds_read_b32 v204, v53 offset:768\n"
    "s_waitcnt lgkmcnt(6)\n"
    "v_fmac_f32 v188, v216, v184\n"
    "v_mfma_f32_16x16x32_bf16 v[232:235], v[4:7], v[84:87], v[232:235]\n"
    "v_mfma_f32_16x16x32_bf16 v[236:239], v[8:11], v[88:91], 0\n"
    "v_fmac_f32 v190, v220, v184\n"
    "v_mfma_f32_16x16x32_bf16 v[236:239], v[12:15], v[92:95], v[236:239]\n"
    "v_mfma_f32_16x16x32_bf16 v[216:219], v[16:19], v[80:83], 0\n"
    "ds_read_b128 v[184:187], v52 offset:8320\n"
    "s_waitcnt lgkmcnt(4)\n"
    "v_fmac_f32 v188, v248, v200\n"
    "v_fmac_f32 v189, v249, v201\n"
    "v_mfma_f32_16x16x32_bf16 v[216:219], v[20:23], v[84:87], v[216:219]\n"
    "v_fmac_f32 v188, v250, v202\n"
    "v_fmac_f32 v189, v251, v203\n"
    "v_mfma_f32_16x16x32_bf16 v[220:223], v[24:27], v[88:91], 0\n"
    "v_fmac_f32 v190, v252, v200\n"
    "v_fmac_f32 v191, v253, v201\n"
    "v_mfma_f32_16x16x32_bf16 v[220:223], v[28:31], v[92:95], v[220:223]\n"
    "v_fmac_f32 v190, v254, v202\n"
    "v_fmac_f32 v191, v255, v203\n"
    "v_mfma_f32_16x16x32_bf16 v[248:251], v[32:35], v[32:35], 0\n"
    "ds_read_b128 v[200:203], v52 offset:4096\n"
    "s_waitcnt lgkmcnt(2)\n"
    "v_fmac_f32 v188, v232, v204\n"
    "v_mfma_f32_16x16x32_bf16 v[248:251], v[36:39], v[36:39], v[248:251]\n"
    "v_mfma_f32_16x16x32_bf16 v[252:255], v[40:43], v[40:43], 0\n"
    "v_fmac_f32 v190, v236, v204\n"
    "v_mfma_f32_16x16x32_bf16 v[252:255], v[44:47], v[44:47], v[252:255]\n"
    "v_mfma_f32_16x16x32_bf16 v[232:235], v[32:35], v[64:67], 0\n"
    "ds_read_b128 v[204:207], v52 offset:6144\n"
    "s_waitcnt lgkmcnt(2)\n"
    "v_fmac_f32 v188, v216, v184\n"
    "v_fmac_f32 v189, v217, v185\n"
    "v_mfma_f32_16x16x32_bf16 v[232:235], v[36:39], v[68:71], v[232:235]\n"
    "v_fmac_f32 v188, v218, v186\n"
    "v_fmac_f32 v189, v219, v187\n"
    "v_mfma_f32_16x16x32_bf16 v[236:239], v[40:43], v[72:75], 0\n"
    "v_fmac_f32 v190, v220, v184\n"
    "v_fmac_f32 v191, v221, v185\n"
    "v_mfma_f32_16x16x32_bf16 v[236:239], v[44:47], v[76:79], v[236:239]\n"
    "v_fmac_f32 v190, v222, v186\n"
    "v_fmac_f32 v191, v223, v187\n"
    "v_mfma_f32_16x16x32_bf16 v[216:219], v[32:35], v[80:83], 0\n"
    "ds_read_b128 v[184:187], v52 offset:9344\n"
    "s_waitcnt lgkmcnt(2)\n"
    "v_fmac_f32 v188, v248, v200\n"
    "v_fmac_f32 v189, v249, v201\n"
    "v_mfma_f32_16x16x32_bf16 v[216:219], v[36:39], v[84:87], v[216:219]\n"
    "v_fmac_f32 v188, v250, v202\n"
    "v_fmac_f32 v189, v251, v203\n"
    "v_mfma_f32_16x16x32_bf16 v[220:223], v[40:43], v[88:91], 0\n"
    "v_fmac_f32 v190, v252, v200\n"
    "v_fmac_f32 v191, v253, v201\n"
    "v_mfma_f32_16x16x32_bf16 v[220:223], v[44:47], v[92:95], v[220:223]\n"
    "v_fmac_f32 v190, v254, v202\n"
    "v_fmac_f32 v191, v255, v203\n"
    "v_mfma_f32_16x16x32_bf16 v[248:251], v[64:67], v[64:67], 0\n"
    "ds_read_b128 v[200:203], v52 offset:7168\n"
    "s_waitcnt lgkmcnt(2)\n"
    "v_fmac_f32 v188, v232, v204\n"
    "v_fmac_f32 v189, v233, v205\n"
    "v_mfma_f32_16x16x32_bf16 v[248:251], v[68:71], v[68:71], v[248:251]\n"
    "v_fmac_f32 v188, v234, v206\n"
    "v_fmac_f32 v189, v235, v207\n"
    "v_mfma_f32_16x16x32_bf16 v[252:255], v[72:75], v[72:75], 0\n"
    "v_fmac_f32 v190, v236, v204\n"
    "v_fmac_f32 v191, v237, v205\n"
    "v_mfma_f32_16x16x32_bf16 v[252:255], v[76:79], v[76:79], v[252:255]\n"
    "v_fmac_f32 v190, v238, v206\n"
    "v_fmac_f32 v191, v239, v207\n"
    "v_mfma_f32_16x16x32_bf16 v[232:235], v[64:67], v[80:83], 0\n"
    "ds_read_b128 v[204:207], v52 offset:10368\n"
    "s_waitcnt lgkmcnt(2)\n"
    "v_fmac_f32 v188, v216, v184\n"
    "v_fmac_f32 v189, v217, v185\n"
    "v_mfma_f32_16x16x32_bf16 v[232:235], v[68:71], v[84:87], v[232:235]\n"
    "v_fmac_f32 v188, v218, v186\n"
    "v_fmac_f32 v189, v219, v187\n"
    "v_mfma_f32_16x16x32_bf16 v[236:239], v[72:75], v[88:91], 0\n"
    "v_fmac_f32 v190, v220, v184\n"
    "v_fmac_f32 v191, v221, v185\n"
    "v_mfma_f32_16x16x32_bf16 v[236:239], v[76:79], v[92:95], v[236:239]\n"
    "v_fmac_f32 v190, v222, v186\n"
    "v_fmac_f32 v191, v223, v187\n"
    "v_mov_b32 v172, 163584\n"
    "ds_read_b32 v173, v172\n"
    "v_mfma_f32_16x16x32_bf16 v[216:219], v[0:3], v[96:99], 0\n"
    "ds_read_b32 v184, v53 offset:1024\n"
    "s_waitcnt lgkmcnt(3)\n"
    "v_fmac_f32 v188, v248, v200\n"
    "v_fmac_f32 v189, v249, v201\n"
    "v_mfma_f32_16x16x32_bf16 v[216:219], v[4:7], v[100:103], v[216:219]\n"
    "v_fmac_f32 v188, v250, v202\n"
    "v_fmac_f32 v189, v251, v203\n"
    "v_mfma_f32_16x16x32_bf16 v[220:223], v[8:11], v[104:107], 0\n"
    "v_fmac_f32 v190, v252, v200\n"
    "v_fmac_f32 v191, v253, v201\n"
    "v_mfma_f32_16x16x32_bf16 v[220:223], v[12:15], v[108:111], v[220:223]\n"
    "v_fmac_f32 v190, v254, v202\n"
    "v_fmac_f32 v191, v255, v203\n"
    "s_waitcnt lgkmcnt(1)\n"
    "L_faA_%=:\n"
    "v_cmp_eq_u32_e64 s[14:15], v172, v173\n"
    "s_nop 3\n"
    "s_cmp_lg_u64 s[14:15], 0\n"
    "s_cbranch_scc1 L_fbA_%=\n"
    "ds_read_b32 v173, v172\n"
    "s_waitcnt lgkmcnt(0)\n"
    "s_branch L_faA_%=\n"
    "L_fbA_%=:\n"
    "v_mfma_f32_16x16x32_bf16 v[248:251], v[16:19], v[96:99], 0\n"
    "ds_read_b128 v[200:203], v52 offset:12416\n"
    "v_fmac_f32 v188, v232, v204\n"
    "v_fmac_f32 v189, v233, v205\n"
    "v_mfma_f32_16x16x32_bf16 v[248:251], v[20:23], v[100:103], v[248:251]\n"
    "v_fmac_f32 v188, v234, v206\n"
    "v_fmac_f32 v189, v235, v207\n"
    "v_mfma_f32_16x16x32_bf16 v[252:255], v[24:27], v[104:107], 0\n"
    "v_fmac_f32 v190, v236, v204\n"
    "v_fmac_f32 v191, v237, v205\n"
    "v_mfma_f32_16x16x32_bf16 v[252:255], v[28:31], v[108:111], v[252:255]\n"
    "v_fmac_f32 v190, v238, v206\n"
    "v_fmac_f32 v191, v239, v207\n"
    "v_mfma_f32_16x16x32_bf16 v[232:235], v[0:3], v[112:115], 0\n"
    "ds_read_b32 v204, v53 offset:1280\n"
    "s_waitcnt lgkmcnt(2)\n"
    "v_fmac_f32 v188, v216, v184\n"
    "v_mfma_f32_16x16x32_bf16 v[232:235], v[4:7], v[116:119], v[232:235]\n"
    "v_mfma_f32_16x16x32_bf16 v[236:239], v[8:11], v[120:123], 0\n"
    "v_fmac_f32 v190, v220, v184\n"
    "v_mfma_f32_16x16x32_bf16 v[236:239], v[12:15], v[124:127], v[236:239]\n"
    "s_add_u32 s65, s57, s59\n"
    "s_add_u32 s65, s65, 524288\n"
    "buffer_load_dwordx4 v[152:155], v58, s[44:47], s65 offen\n"
    "v_mfma_f32_16x16x32_bf16 v[216:219], v[16:19], v[112:115], 0\n"
    "ds_read_b128 v[184:187], v52 offset:17664\n"
    "s_waitcnt lgkmcnt(2)\n"
    "v_fmac_f32 v188, v248, v200\n"
    "v_fmac_f32 v189, v249, v201\n"
    "v_mfma_f32_16x16x32_bf16 v[216:219], v[20:23], v[116:119], v[216:219]\n"
    "v_fmac_f32 v188, v250, v202\n"
    "v_fmac_f32 v189, v251, v203\n"
    "v_mfma_f32_16x16x32_bf16 v[220:223], v[24:27], v[120:123], 0\n"
    "v_fmac_f32 v190, v252, v200\n"
    "v_fmac_f32 v191, v253, v201\n"
    "v_mfma_f32_16x16x32_bf16 v[220:223], v[28:31], v[124:127], v[220:223]\n"
    "v_fmac_f32 v190, v254, v202\n"
    "v_fmac_f32 v191, v255, v203\n"
    "v_mfma_f32_16x16x32_bf16 v[248:251], v[32:35], v[96:99], 0\n"
    "ds_read_b128 v[200:203], v52 offset:13440\n"
    "s_waitcnt lgkmcnt(2)\n"
    "v_fmac_f32 v188, v232, v204\n"
    "v_mfma_f32_16x16x32_bf16 v[248:251], v[36:39], v[100:103], v[248:251]\n"
    "v_mfma_f32_16x16x32_bf16 v[252:255], v[40:43], v[104:107], 0\n"
    "v_fmac_f32 v190, v236, v204\n"
    "v_mfma_f32_16x16x32_bf16 v[252:255], v[44:47], v[108:111], v[252:255]\n"
    "v_mfma_f32_16x16x32_bf16 v[232:235], v[32:35], v[112:115], 0\n"
    "ds_read_b128 v[204:207], v52 offset:18688\n"
    "s_waitcnt lgkmcnt(2)\n"
    "v_fmac_f32 v188, v216, v184\n"
    "v_fmac_f32 v189, v217, v185\n"
    "v_mfma_f32_16x16x32_bf16 v[232:235], v[36:39], v[116:119], v[232:235]\n"
    "v_fmac_f32 v188, v218, v186\n"
    "v_fmac_f32 v189, v219, v187\n"
    "v_mfma_f32_16x16x32_bf16 v[236:239], v[40:43], v[120:123], 0\n"
    "v_fmac_f32 v190, v220, v184\n"
    "v_fmac_f32 v191, v221, v185\n"
    "v_mfma_f32_16x16x32_bf16 v[236:239], v[44:47], v[124:127], v[236:239]\n"
    "v_fmac_f32 v190, v222, v186\n"
    "v_fmac_f32 v191, v223, v187\n"
    "v_mfma_f32_16x16x32_bf16 v[216:219], v[64:67], v[96:99], 0\n"
    "ds_read_b128 v[184:187], v52 offset:14464\n"
    "s_waitcnt lgkmcnt(2)\n"
    "v_fmac_f32 v188, v248, v200\n"
    "v_fmac_f32 v189, v249, v201\n"
    "v_mfma_f32_16x16x32_bf16 v[216:219], v[68:71], v[100:103], v[216:219]\n"
    "v_fmac_f32 v188, v250, v202\n"
    "v_fmac_f32 v189, v251, v203\n"
    "v_mfma_f32_16x16x32_bf16 v[220:223], v[72:75], v[104:107], 0\n"
    "v_fmac_f32 v190, v252, v200\n"
    "v_fmac_f32 v191, v253, v201\n"
    "v_mfma_f32_16x16x32_bf16 v[220:223], v[76:79], v[108:111], v[220:223]\n"
    "v_fmac_f32 v190, v254, v202\n"
    "v_fmac_f32 v191, v255, v203\n"
    "v_mfma_f32_16x16x32_bf16 v[248:251], v[64:67], v[112:115], 0\n"
    "ds_read_b128 v[200:203], v52 offset:19712\n"
    "s_waitcnt lgkmcnt(2)\n"
    "v_fmac_f32 v188, v232, v204\n"
    "v_fmac_f32 v189, v233, v205\n"
    "v_mfma_f32_16x16x32_bf16 v[248:251], v[68:71], v[116:119], v[248:251]\n"
    "v_fmac_f32 v188, v234, v206\n"
    "v_fmac_f32 v189, v235, v207\n"
    "v_mfma_f32_16x16x32_bf16 v[252:255], v[72:75], v[120:123], 0\n"
    "v_fmac_f32 v190, v236, v204\n"
    "v_fmac_f32 v191, v237, v205\n"
    "v_mfma_f32_16x16x32_bf16 v[252:255], v[76:79], v[124:127], v[252:255]\n"
    "v_fmac_f32 v190, v238, v206\n"
    "v_fmac_f32 v191, v239, v207\n"
    "s_waitcnt lgkmcnt(0)\n"
    "s_barrier\n"
    "s_add_u32 m0, s54, 0\n"
    "s_add_u32 s16, s56, 0\n"
    "buffer_load_dwordx4 v48, s[36:39], s16 offen lds\n"
    "s_add_u32 m0, s54, 1024\n"
    "s_nop 0\n"
    "buffer_load_dwordx4 v49, s[36:39], s16 offen lds\n"
    "s_add_u32 m0, s54, 2048\n"
    "s_add_u32 s16, s56, 4096\n"
    "buffer_load_dwordx4 v48, s[36:39], s16 offen lds\n"
    "s_add_u32 m0, s54, 3072\n"
    "s_nop 0\n"
    "buffer_load_dwordx4 v49, s[36:39], s16 offen lds\n"
    "s_add_u32 m0, s54, 4096\n"
    "s_add_u32 s16, s56, 8192\n"
    "buffer_load_dwordx4 v48, s[36:39], s16 offen lds\n"
    "s_add_u32 m0, s54, 5120\n"
    "s_nop 0\n"
    "buffer_load_dwordx4 v49, s[36:39], s16 offen lds\n"
    "s_add_u32 m0, s54, 6144\n"
    "s_add_u32 s16, s56, 12288\n"
    "buffer_load_dwordx4 v48, s[36:39], s16 offen lds\n"
    "s_add_u32 m0, s54, 7168\n"
    "s_nop 0\n"
    "buffer_load_dwordx4 v49, s[36:39], s16 offen lds\n"
    "s_add_u32 m0, s54, 98304\n"
    "s_add_u32 s16, s56, 147456\n"
    "buffer_load_dwordx4 v48, s[36:39], s16 offen lds\n"
    "s_add_u32 m0, s54, 99328\n"
    "s_nop 0\n"
    "buffer_load_dwordx4 v49, s[36:39], s16 offen lds\n"
    "s_add_u32 m0, s54, 100352\n"
    "s_add_u32 s16, s56, 151552\n"
    "buffer_load_dwordx4 v48, s[36:39], s16 offen lds\n"
    "s_add_u32 m0, s54, 101376\n"
    "s_nop 0\n"
    "buffer_load_dwordx4 v49, s[36:39], s16 offen lds\n"
    "s_add_u32 m0, s54, 102400\n"
    "s_add_u32 s16, s56, 155648\n"
    "buffer_load_dwordx4 v48, s[36:39], s16 offen lds\n"
    "s_add_u32 m0, s54, 103424\n"
    "s_nop 0\n"
    "buffer_load_dwordx4 v49, s[36:39], s16 offen lds\n"
    "s_add_u32 m0, s54, 104448\n"
    "s_add_u32 s16, s56, 159744\n"
    "buffer_load_dwordx4 v48, s[36:39], s16 offen lds\n"
    "s_add_u32 m0, s54, 105472\n"
    "s_nop 0\n"
    "buffer_load_dwordx4 v49, s[36:39], s16 offen lds\n"
    "s_add_u32 m0, s54, 65536\n"
    "s_add_u32 s16, s56, 16384\n"
    "buffer_load_dwordx4 v48, s[36:39], s16 offen lds\n"
    "s_add_u32 m0, s54, 66560\n"
    "s_nop 0\n"
    "buffer_load_dwordx4 v49, s[36:39], s16 offen lds\n"
    "s_add_u32 m0, s54, 67584\n"
    "s_add_u32 s16, s56, 20480\n"
    "buffer_load_dwordx4 v48, s[36:39], s16 offen lds\n"
    "s_add_u32 m0, s54, 68608\n"
    "s_nop 0\n"
    "buffer_load_dwordx4 v49, s[36:39], s16 offen lds\n"
    "s_add_u32 m0, s54, 69632\n"
    "s_add_u32 s16, s56, 24576\n"
    "buffer_load_dwordx4 v48, s[36:39], s16 offen lds\n"
    "s_add_u32 m0, s54, 70656\n"
    "s_nop 0\n"
    "buffer_load_dwordx4 v49, s[36:39], s16 offen lds\n"
    "s_add_u32 m0, s54, 71680\n"
    "s_add_u32 s16, s56, 28672\n"
    "buffer_load_dwordx4 v48, s[36:39], s16 offen lds\n"
    "s_add_u32 m0, s54, 72704\n"
    "s_nop 0\n"
    "buffer_load_dwordx4 v49, s[36:39], s16 offen lds\n"
    "v_mfma_f32_16x16x32_bf16 v[232:235], v[80:83], v[80:83], 0\n"
    "ds_read_b128 v[204:207], v52 offset:11392\n"
    "v_fmac_f32 v188, v216, v184\n"
    "v_fmac_f32 v189, v217, v185\n"
    "v_mfma_f32_16x16x32_bf16 v[232:235], v[84:87], v[84:87], v[232:235]\n"
    "v_fmac_f32 v188, v218, v186\n"
    "v_fmac_f32 v189, v219, v187\n"
    "v_mfma_f32_16x16x32_bf16 v[236:239], v[88:91], v[88:91], 0\n"
    "v_fmac_f32 v190, v220, v184\n"
    "v_fmac_f32 v191, v221, v185\n"
    "v_mfma_f32_16x16x32_bf16 v[236:239], v[92:95], v[92:95], v[236:239]\n"
    "v_fmac_f32 v190, v222, v186\n"
    "v_fmac_f32 v191, v223, v187\n"
    "v_mfma_f32_16x16x32_bf16 v[216:219], v[80:83], v[96:99], 0\n"
    "ds_read_b128 v[184:187], v52 offset:15488\n"
    "v_fmac_f32 v188, v248, v200\n"
    "v_fmac_f32 v189, v249, v201\n"
    "v_mfma_f32_16x16x32_bf16 v[216:219], v[84:87], v[100:103], v[216:219]\n"
    "v_fmac_f32 v188, v250, v202\n"
    "v_fmac_f32 v189, v251, v203\n"
    "v_mfma_f32_16x16x32_bf16 v[220:223], v[88:91], v[104:107], 0\n"
    "v_fmac_f32 v190, v252, v200\n"
    "v_fmac_f32 v191, v253, v201\n"
    "v_mfma_f32_16x16x32_bf16 v[220:223], v[92:95], v[108:111], v[220:223]\n"
    "v_fmac_f32 v190, v254, v202\n"
    "v_fmac_f32 v191, v255, v203\n"
    "v_mfma_f32_16x16x32_bf16 v[248:251], v[80:83], v[112:115], 0\n"
    "ds_read_b128 v[200:203], v52 offset:20736\n"
    "s_waitcnt lgkmcnt(2)\n"
    "v_fmac_f32 v188, v232, v204\n"
    "v_fmac_f32 v189, v233, v205\n"
    "ds_read_b64_tr_b16 v[0:1], v56\n"
    "ds_read_b64_tr_b16 v[2:3], v56 offset:64\n"
    "v_mfma_f32_16x16x32_bf16 v[248:251], v[84:87], v[116:119], v[248:251]\n"
    "v_fmac_f32 v188, v234, v206\n"
    "v_fmac_f32 v189, v235, v207\n"
    "ds_read_b64_tr_b16 v[4:5], v56 offset:512\n"
    "ds_read_b64_tr_b16 v[6:7], v56 offset:576\n"
    "v_mfma_f32_16x16x32_bf16 v[252:255], v[88:91], v[120:123], 0\n"
    "v_fmac_f32 v190, v236, v204\n"
    "v_fmac_f32 v191, v237, v205\n"
    "ds_read_b64_tr_b16 v[8:9], v56 offset:1024\n"
    "ds_read_b64_tr_b16 v[10:11], v56 offset:1088\n"
    "v_mfma_f32_16x16x32_bf16 v[252:255], v[92:95], v[124:127], v[252:255]\n"
    "v_fmac_f32 v190, v238, v206\n"
    "v_fmac_f32 v191, v239, v207\n"
    "ds_read_b64_tr_b16 v[12:13], v56 offset:1536\n"
    "ds_read_b64_tr_b16 v[14:15], v56 offset:1600\n"
    "v_mfma_f32_16x16x32_bf16 v[232:235], v[96:99], v[96:99], 0\n"
    "ds_read_b128 v[204:207], v52 offset:16640\n"
    "s_waitcnt lgkmcnt(10)\n"
    "v_fmac_f32 v188, v216, v184\n"
    "v_fmac_f32 v189, v217, v185\n"
    "ds_read_b64_tr_b16 v[16:17], v56 offset:8\n"
    "ds_read_b64_tr_b16 v[18:19], v56 offset:72\n"
    "v_mfma_f32_16x16x32_bf16 v[232:235], v[100:103], v[100:103], v[232:235]\n"
    "v_fmac_f32 v188, v218, v186\n"
    "v_fmac_f32 v189, v219, v187\n"
    "ds_read_b64_tr_b16 v[20:21], v56 offset:520\n"
    "ds_read_b64_tr_b16 v[22:23], v56 offset:584\n"
    "v_mfma_f32_16x16x32_bf16 v[236:239], v[104:107], v[104:107], 0\n"
    "v_fmac_f32 v190, v220, v184\n"
    "v_fmac_f32 v191, v221, v185\n"
    "ds_read_b64_tr_b16 v[24:25], v56 offset:1032\n"
    "ds_read_b64_tr_b16 v[26:27], v56 offset:1096\n"
    "v_mfma_f32_16x16x32_bf16 v[236:239], v[108:111], v[108:111], v[236:239]\n"
    "v_fmac_f32 v190, v222, v186\n"
    "v_fmac_f32 v191, v223, v187\n"
    "ds_read_b64_tr_b16 v[28:29], v56 offset:1544\n"
    "ds_read_b64_tr_b16 v[30:31], v56 offset:1608\n"
    "v_mfma_f32_16x16x32_bf16 v[216:219], v[96:99], v[112:115], 0\n"
    "ds_read_b128 v[184:187], v52 offset:21760\n"
    "s_waitcnt lgkmcnt(15)\n"
    "v_fmac_f32 v188, v248, v200\n"
    "v_fmac_f32 v189, v249, v201\n"
    "v_mfma_f32_16x16x32_bf16 v[216:219], v[100:103], v[116:119], v[216:219]\n"
    "v_fmac_f32 v188, v250, v202\n"
    "v_fmac_f32 v189, v251, v203\n"
    "v_mfma_f32_16x16x32_bf16 v[220:223], v[104:107], v[120:123], 0\n"
    "v_fmac_f32 v190, v252, v200\n"
    "v_fmac_f32 v191, v253, v201\n"
    "v_mfma_f32_16x16x32_bf16 v[220:223], v[108:111], v[124:127], v[220:223]\n"
    "v_fmac_f32 v190, v254, v202\n"
    "v_fmac_f32 v191, v255, v203\n"
    "v_mov_b32 v156, -1\n"
    "v_mov_b32 v157, -1\n"
    "v_mov_b32 v158, -1\n"
    "v_mov_b32 v159, -1\n"
    "buffer_store_dwordx4 v[156:159], v57, s[48:51], s18 offen sc1\n"
    "v_mfma_f32_16x16x32_bf16 v[248:251], v[112:115], v[112:115], 0\n"
    "ds_read_b128 v[200:203], v52 offset:22784\n"
    "s_waitcnt lgkmcnt(10)\n"
    "v_fmac_f32 v188, v232, v204\n"
    "v_fmac_f32 v189, v233, v205\n"
    "v_mfma_f32_16x16x32_bf16 v[248:251], v[116:119], v[116:119], v[248:251]\n"
    "v_fmac_f32 v188, v234, v206\n"
    "v_fmac_f32 v189, v235, v207\n"
    "v_mfma_f32_16x16x32_bf16 v[252:255], v[120:123], v[120:123], 0\n"
    "v_fmac_f32 v190, v236, v204\n"
    "v_fmac_f32 v191, v237, v205\n"
    "v_mfma_f32_16x16x32_bf16 v[252:255], v[124:127], v[124:127], v[252:255]\n"
    "v_fmac_f32 v190, v238, v206\n"
    "v_fmac_f32 v191, v239, v207\n"
    "s_waitcnt vmcnt(23)\n"
    "ds_read_b128 v[136:139], v50\n"
    "ds_read_b128 v[140:143], v51\n"
    "s_waitcnt lgkmcnt(3)\n"
    "v_fmac_f32 v188, v216, v184\n"
    "v_fmac_f32 v189, v217, v185\n"
    "v_fmac_f32 v188, v218, v186\n"
    "v_fmac_f32 v189, v219, v187\n"
    "v_fmac_f32 v190, v220, v184\n"
    "v_fmac_f32 v191, v221, v185\n"
    "v_fmac_f32 v190, v222, v186\n"
    "v_fmac_f32 v191, v223, v187\n"
    "v_mov_b32 v120, v152\n"
    "v_mov_b32 v121, v153\n"
    "v_mov_b32 v122, v154\n"
    "v_mov_b32 v123, v155\n"
    "s_waitcnt lgkmcnt(2)\n"
    "v_fmac_f32 v188, v248, v200\n"
    "v_fmac_f32 v189, v249, v201\n"
    "v_fmac_f32 v188, v250, v202\n"
    "v_fmac_f32 v189, v251, v203\n"
    "v_fmac_f32 v190, v252, v200\n"
    "v_fmac_f32 v191, v253, v201\n"
    "v_fmac_f32 v190, v254, v202\n"
    "v_fmac_f32 v191, v255, v203\n"
    "s_lshl_b32 s67, s11, 3\n"
    "s_add_u32 s67, s67, s10\n"
    "s_lshl_b32 s67, s67, 9\n"
    "buffer_load_dwordx2 v[48:49], v55, s[48:51], s67 offen sc1\n"
    "s_add_u32 s57, s57, 1048576\n"
    "s_mul_i32 s56, s11, 56\n"
    "s_add_u32 s56, s56, s10\n"
    "s_lshl_b32 s56, s56, 12\n"
    "s_add_u32 s56, s56, 262144\n"
    "s_mov_b32 m0, s54\n"
    "s_setprio 1\n"
    "s_waitcnt vmcnt(22)\n"
    "ds_read_b128 v[152:155], v50 offset:2048\n"
    "ds_read_b128 v[156:159], v51 offset:2048\n"
    "v_mfma_f32_16x16x32_bf16 v[32:35], v[128:131], v[0:3], 0\n"
    "v_add_f32 v188, v188, v189\n"
    "v_add_f32 v190, v190, v191\n"
    "v_mfma_f32_16x16x32_bf16 v[32:35], v[132:135], v[4:7], v[32:35]\n"
    "v_add_u32 v124, 0x10000, v50\n"
    "v_add_u32 v125, 0x10000, v51\n"
    "s_waitcnt lgkmcnt(2)\n"
    "v_mfma_f32_16x16x32_bf16 v[32:35], v[136:139], v[8:11], v[32:35]\n"
    "v_mov_b32 v126, v54\n"
    "v_permlane32_swap_b32 v188, v190\n"
    "v_mfma_f32_16x16x32_bf16 v[32:35], v[140:143], v[12:15], v[32:35]\n"
    "v_add_f32 v188, v188, v190\n"
    "v_mov_b32 v189, v188\n"
    "v_mfma_f32_16x16x32_bf16 v[36:39], v[128:131], v[16:19], 0\n"
    "s_nop 0\n"
    "v_permlane16_swap_b32 v188, v189\n"
    "v_add_f32 v188, v188, v189\n"
    "v_mfma_f32_16x16x32_bf16 v[36:39], v[132:135], v[20:23], v[36:39]\n"
    "s_nop 0\n"
    "v_add_f32_dpp v188, v188, v188 row_ror:8 row_mask:0xf bank_mask:0xf\n"
    "s_nop 1\n"
    "v_add_f32_dpp v188, v188, v188 row_ror:4 row_mask:0xf bank_mask:0xf\n"
    "v_mfma_f32_16x16x32_bf16 v[36:39], v[136:139], v[24:27], v[36:39]\n"
    "s_nop 0\n"
    "v_add_f32_dpp v188, v188, v188 row_ror:2 row_mask:0xf bank_mask:0xf\n"
    "s_nop 1\n"
    "v_add_f32_dpp v188, v188, v188 row_ror:1 row_mask:0xf bank_mask:0xf\n"
    "v_mfma_f32_16x16x32_bf16 v[36:39], v[140:143], v[28:31], v[36:39]\n"
    "v_lshl_add_u32 v189, s10, 5, v55\n"
    "v_lshrrev_b32 v189, 3, v189\n"
    "s_waitcnt vmcnt(20)\n"
    "ds_read_b128 v[168:171], v50 offset:4096\n"
    "ds_read_b128 v[172:175], v51 offset:4096\n"
    "v_mfma_f32_16x16x32_bf16 v[40:43], v[144:147], v[0:3], 0\n"
    "v_add_u32 v189, 163264, v189\n"
    "v_mfma_f32_16x16x32_bf16 v[40:43], v[148:151], v[4:7], v[40:43]\n"
    "s_mov_b32 exec_lo, 1\n"
    "s_mov_b32 exec_hi, 1\n"
    "ds_write_b32 v189, v188\n"
    "s_mov_b64 exec, -1\n"
    "s_waitcnt lgkmcnt(3)\n"
    "v_mfma_f32_16x16x32_bf16 v[40:43], v[152:155], v[8:11], v[40:43]\n"
    "v_mov_b64 v[64:65], 0\n"
    "v_mfma_f32_16x16x32_bf16 v[40:43], v[156:159], v[12:15], v[40:43]\n"
    "v_mfma_f32_16x16x32_bf16 v[44:47], v[144:147], v[16:19], 0\n"
    "v_cvt_pk_bf16_f32 v36, v32, v36\n"
    "v_mov_b64 v[66:67], 0\n"
    "v_mfma_f32_16x16x32_bf16 v[44:47], v[148:151], v[20:23], v[44:47]\n"
    "v_cvt_pk_bf16_f32 v37, v33, v37\n"
    "v_mfma_f32_16x16x32_bf16 v[44:47], v[152:155], v[24:27], v[44:47]\n"
    "v_cvt_pk_bf16_f32 v38, v34, v38\n"
    "v_mov_b64 v[68:69], 0\n"
    "v_mfma_f32_16x16x32_bf16 v[44:47], v[156:159], v[28:31], v[44:47]\n"
    "v_cvt_pk_bf16_f32 v39, v35, v39\n"
    "s_waitcnt vmcnt(18)\n"
    "ds_read_b128 v[184:187], v50 offset:6144\n"
    "ds_read_b128 v[188:191], v51 offset:6144\n"
    "v_mfma_f32_16x16x32_bf16 v[32:35], v[160:163], v[0:3], 0\n"
    "ds_write_addtid_b32 v36 offset:0\n"
    "v_mov_b64 v[70:71], 0\n"
    "v_mfma_f32_16x16x32_bf16 v[32:35], v[164:167], v[4:7], v[32:35]\n"
    "ds_write_addtid_b32 v37 offset:256\n"
    "s_waitcnt lgkmcnt(5)\n"
    "v_mfma_f32_16x16x32_bf16 v[32:35], v[168:171], v[8:11], v[32:35]\n"
    "ds_write_addtid_b32 v38 offset:512\n"
    "v_mov_b64 v[72:73], 0\n"
    "v_mfma_f32_16x16x32_bf16 v[32:35], v[172:175], v[12:15], v[32:35]\n"
    "ds_write_addtid_b32 v39 offset:768\n"
    "v_mfma_f32_16x16x32_bf16 v[36:39], v[160:163], v[16:19], 0\n"
    "v_cvt_pk_bf16_f32 v44, v40, v44\n"
    "v_mov_b64 v[74:75], 0\n"
    "v_mfma_f32_16x16x32_bf16 v[36:39], v[164:167], v[20:23], v[36:39]\n"
    "v_cvt_pk_bf16_f32 v45, v41, v45\n"
    "v_mfma_f32_16x16x32_bf16 v[36:39], v[168:171], v[24:27], v[36:39]\n"
    "v_cvt_pk_bf16_f32 v46, v42, v46\n"
    "v_mov_b64 v[76:77], 0\n"
    "v_mfma_f32_16x16x32_bf16 v[36:39], v[172:175], v[28:31], v[36:39]\n"
    "v_cvt_pk_bf16_f32 v47, v43, v47\n"
    "s_waitcnt vmcnt(8)\n"
    "ds_read_b128 v[200:203], v124\n"
    "ds_read_b128 v[204:207], v125\n"
    "v_mfma_f32_16x16x32_bf16 v[40:43], v[176:179], v[0:3], 0\n"
    "ds_write_addtid_b32 v44 offset:1024\n"
    "v_mov_b64 v[78:79], 0\n"
    "v_mfma_f32_16x16x32_bf16 v[40:43], v[180:183], v[4:7], v[40:43]\n"
    "ds_write_addtid_b32 v45 offset:1280\n"
    "s_waitcnt lgkmcnt(8)\n"
    "v_mfma_f32_16x16x32_bf16 v[40:43], v[184:187], v[8:11], v[40:43]\n"
    "ds_write_addtid_b32 v46 offset:1536\n"
    "v_mov_b64 v[80:81], 0\n"
    "v_mfma_f32_16x16x32_bf16 v[40:43], v[188:191], v[12:15], v[40:43]\n"
    "ds_write_addtid_b32 v47 offset:1792\n"
    "v_mfma_f32_16x16x32_bf16 v[44:47], v[176:179], v[16:19], 0\n"
    "v_cvt_pk_bf16_f32 v36, v32, v36\n"
    "v_mov_b64 v[82:83], 0\n"
    "v_mfma_f32_16x16x32_bf16 v[44:47], v[180:183], v[20:23], v[44:47]\n"
    "v_cvt_pk_bf16_f32 v37, v33, v37\n"
    "v_mfma_f32_16x16x32_bf16 v[44:47], v[184:187], v[24:27], v[44:47]\n"
    "v_cvt_pk_bf16_f32 v38, v34, v38\n"
    "v_mov_b64 v[84:85], 0\n"
    "v_mfma_f32_16x16x32_bf16 v[44:47], v[188:191], v[28:31], v[44:47]\n"
    "v_cvt_pk_bf16_f32 v39, v35, v39\n"
    "s_barrier\n"
    "s_add_u32 s65, s53, 8320\n"
    "v_add_u32 v127, s65, v57\n"
    "ds_write2_b64 v127, v[120:121], v[122:123] offset1:1\n"
    "s_add_u32 s65, s57, s59\n"
    "s_add_u32 m0, s53, 16640\n"
    "s_nop 0\n"
    "buffer_load_dwordx4 v58, s[44:47], s65 offen lds\n"
    "s_mov_b32 m0, s54\n"
    "s_mul_i32 s66, s10, 800\n"
    "s_add_u32 m0, s66, 156032\n"
    "s_mov_b32 exec_hi, 0x3ffff\n"
    "buffer_load_dwordx4 v126, s[4:7], s61 offen lds\n"
    "s_mov_b32 exec_hi, -1\n"
    "s_mov_b32 m0, s54\n"
    "s_waitcnt vmcnt(8)\n"
    "ds_read_b128 v[216:219], v124 offset:2048\n"
    "ds_read_b128 v[220:223], v125 offset:2048\n"
    "v_mfma_f32_16x16x32_bf16 v[32:35], v[192:195], v[0:3], 0\n"
    "ds_write_addtid_b32 v36 offset:2048\n"
    "v_mov_b64 v[86:87], 0\n"
    "v_mfma_f32_16x16x32_bf16 v[32:35], v[196:199], v[4:7], v[32:35]\n"
    "ds_write_addtid_b32 v37 offset:2304\n"
    "s_waitcnt lgkmcnt(9)\n"
    "v_mfma_f32_16x16x32_bf16 v[32:35], v[200:203], v[8:11], v[32:35]\n"
    "ds_write_addtid_b32 v38 offset:2560\n"
    "v_mov_b64 v[88:89], 0\n"
    "v_mfma_f32_16x16x32_bf16 v[32:35], v[204:207], v[12:15], v[32:35]\n"
    "ds_write_addtid_b32 v39 offset:2816\n"
    "v_mfma_f32_16x16x32_bf16 v[36:39], v[192:195], v[16:19], 0\n"
    "v_cvt_pk_bf16_f32 v44, v40, v44\n"
    "v_mov_b64 v[90:91], 0\n"
    "v_mfma_f32_16x16x32_bf16 v[36:39], v[196:199], v[20:23], v[36:39]\n"
    "v_cvt_pk_bf16_f32 v45, v41, v45\n"
    "v_mfma_f32_16x16x32_bf16 v[36:39], v[200:203], v[24:27], v[36:39]\n"
    "v_cvt_pk_bf16_f32 v46, v42, v46\n"
    "v_mov_b64 v[92:93], 0\n"
    "v_mfma_f32_16x16x32_bf16 v[36:39], v[204:207], v[28:31], v[36:39]\n"
    "v_cvt_pk_bf16_f32 v47, v43, v47\n"
    "s_waitcnt vmcnt(6)\n"
    "ds_read_b128 v[232:235], v124 offset:4096\n"
    "ds_read_b128 v[236:239], v125 offset:4096\n"
    "v_mfma_f32_16x16x32_bf16 v[40:43], v[208:211], v[0:3], 0\n"
    "ds_write_addtid_b32 v44 offset:3072\n"
    "v_mov_b64 v[94:95], 0\n"
    "v_mfma_f32_16x16x32_bf16 v[40:43], v[212:215], v[4:7], v[40:43]\n"
    "ds_write_addtid_b32 v45 offset:3328\n"
    "s_waitcnt lgkmcnt(8)\n"
    "v_mfma_f32_16x16x32_bf16 v[40:43], v[216:219], v[8:11], v[40:43]\n"
    "ds_write_addtid_b32 v46 offset:3584\n"
    "v_mov_b64 v[96:97], 0\n"
    "v_mfma_f32_16x16x32_bf16 v[40:43], v[220:223], v[12:15], v[40:43]\n"
    "ds_write_addtid_b32 v47 offset:3840\n"
    "v_mfma_f32_16x16x32_bf16 v[44:47], v[208:211], v[16:19], 0\n"
    "v_cvt_pk_bf16_f32 v36, v32, v36\n"
    "v_mov_b64 v[98:99], 0\n"
    "v_mfma_f32_16x16x32_bf16 v[44:47], v[212:215], v[20:23], v[44:47]\n"
    "v_cvt_pk_bf16_f32 v37, v33, v37\n"
    "v_mfma_f32_16x16x32_bf16 v[44:47], v[216:219], v[24:27], v[44:47]\n"
    "v_cvt_pk_bf16_f32 v38, v34, v38\n"
    "v_mov_b64 v[100:101], 0\n"
    "v_mfma_f32_16x16x32_bf16 v[44:47], v[220:223], v[28:31], v[44:47]\n"
    "v_cvt_pk_bf16_f32 v39, v35, v39\n"
    "s_waitcnt vmcnt(4)\n"
    "ds_read_b128 v[248:251], v124 offset:6144\n"
    "ds_read_b128 v[252:255], v125 offset:6144\n"
    "v_mfma_f32_16x16x32_bf16 v[32:35], v[224:227], v[0:3], 0\n"
    "ds_write_addtid_b32 v36 offset:4096\n"
    "v_mov_b64 v[102:103], 0\n"
    "v_mfma_f32_16x16x32_bf16 v[32:35], v[228:231], v[4:7], v[32:35]\n"
    "ds_write_addtid_b32 v37 offset:4352\n"
    "s_waitcnt lgkmcnt(8)\n"
    "v_mfma_f32_16x16x32_bf16 v[32:35], v[232:235], v[8:11], v[32:35]\n"
    "ds_write_addtid_b32 v38 offset:4608\n"
    "v_mov_b64 v[104:105], 0\n"
    "v_mfma_f32_16x16x32_bf16 v[32:35], v[236:239], v[12:15], v[32:35]\n"
    "ds_write_addtid_b32 v39 offset:4864\n"
    "v_mfma_f32_16x16x32_bf16 v[36:39], v[224:227], v[16:19], 0\n"
    "v_cvt_pk_bf16_f32 v44, v40, v44\n"
    "v_mov_b64 v[106:107], 0\n"
    "v_mfma_f32_16x16x32_bf16 v[36:39], v[228:231], v[20:23], v[36:39]\n"
    "v_cvt_pk_bf16_f32 v45, v41, v45\n"
    "v_mfma_f32_16x16x32_bf16 v[36:39], v[232:235], v[24:27], v[36:39]\n"
    "v_cvt_pk_bf16_f32 v46, v42, v46\n"
    "v_mov_b64 v[108:109], 0\n"
    "v_mfma_f32_16x16x32_bf16 v[36:39], v[236:239], v[28:31], v[36:39]\n"
    "v_cvt_pk_bf16_f32 v47, v43, v47\n"
    "s_add_u32 s65, s57, s59\n"
    "s_xor_b32 s65, s65, 262144\n"
    "s_bitcmp1_b32 s59, 18\n"
    "s_cselect_b32 s66, -8320, 0\n"
    "s_add_u32 s66, s66, s53\n"
    "s_add_u32 m0, s66, 20800\n"
    "s_nop 0\n"
    "buffer_load_dwordx4 v58, s[44:47], s65 offen lds\n"
    "s_mov_b32 m0, s54\n"
    "v_mfma_f32_16x16x32_bf16 v[40:43], v[240:243], v[0:3], 0\n"
    "ds_write_addtid_b32 v44 offset:5120\n"
    "v_mov_b64 v[110:111], 0\n"
    "v_mfma_f32_16x16x32_bf16 v[40:43], v[244:247], v[4:7], v[40:43]\n"
    "ds_write_addtid_b32 v45 offset:5376\n"
    "s_waitcnt lgkmcnt(6)\n"
    "v_mfma_f32_16x16x32_bf16 v[40:43], v[248:251], v[8:11], v[40:43]\n"
    "ds_write_addtid_b32 v46 offset:5632\n"
    "v_mov_b64 v[112:113], 0\n"
    "v_mfma_f32_16x16x32_bf16 v[40:43], v[252:255], v[12:15], v[40:43]\n"
    "ds_write_addtid_b32 v47 offset:5888\n"
    "v_mfma_f32_16x16x32_bf16 v[44:47], v[240:243], v[16:19], 0\n"
    "v_cvt_pk_bf16_f32 v36, v32, v36\n"
    "v_mov_b64 v[114:115], 0\n"
    "v_mfma_f32_16x16x32_bf16 v[44:47], v[244:247], v[20:23], v[44:47]\n"
    "v_cvt_pk_bf16_f32 v37, v33, v37\n"
    "v_mfma_f32_16x16x32_bf16 v[44:47], v[248:251], v[24:27], v[44:47]\n"
    "v_cvt_pk_bf16_f32 v38, v34, v38\n"
    "v_mov_b64 v[116:117], 0\n"
    "v_mfma_f32_16x16x32_bf16 v[44:47], v[252:255], v[28:31], v[44:47]\n"
    "v_cvt_pk_bf16_f32 v39, v35, v39\n"
    "ds_write_addtid_b32 v36 offset:6144\n"
    "v_mov_b64 v[118:119], 0\n"
    "ds_write_addtid_b32 v37 offset:6400\n"
    "ds_write_addtid_b32 v38 offset:6656\n"
    "ds_write_addtid_b32 v39 offset:6912\n"
    "s_nop 1\n"
    "v_cvt_pk_bf16_f32 v44, v40, v44\n"
    "v_cvt_pk_bf16_f32 v45, v41, v45\n"
    "v_cvt_pk_bf16_f32 v46, v42, v46\n"
    "v_cvt_pk_bf16_f32 v47, v43, v47\n"
    "s_waitcnt vmcnt(3)\n"
    "s_mov_b32 s66, 65536\n"
    "L_p1Aa_%=:\n"
    "v_cmp_ne_u64_e64 s[14:15], v[48:49], s[12:13]\n"
    "s_nop 3\n"
    "s_or_b32 s14, s14, s15\n"
    "s_and_b32 s14, s14, 0xf0f0f0f0\n"
    "s_cbranch_scc0 L_p1Ab_%=\n"
    "s_sleep 2\n"
    "buffer_load_dwordx2 v[48:49], v55, s[48:51], s67 offen sc1\n"
    "s_waitcnt vmcnt(0)\n"
    "s_sub_u32 s66, s66, 1\n"
    "s_cmp_lg_u32 s66, 0\n"
    "s_cbranch_scc1 L_p1Aa_%=\n"
    "L_p1Ab_%=:\n"
    "ds_write_addtid_b32 v44 offset:7168\n"
    "ds_write_addtid_b32 v45 offset:7424\n"
    "ds_write_addtid_b32 v46 offset:7680\n"
    "ds_write_addtid_b32 v47 offset:7936\n"
    "v_mov_b64 v[120:121], 0\n"
    "v_mov_b64 v[122:123], 0\n"
    "v_mov_b64 v[124:125], 0\n"
    "v_mov_b64 v[126:127], 0\n"
    "s_add_u32 s57, s57, 524288\n"
    "s_add_u32 s58, s57, 524288\n"
    "s_mov_b32 s61, s53\n"
    "s_mov_b32 s62, 0\n"
    "s_mov_b32 s63, 1\n"
    "s_mov_b32 s64, -1\n"
    "s_add_u32 s31, s18, 1024\n"
    "v_add_u32 v56, 8320, v56\n"
    "s_mov_b32 s43, 0xffff\n"
    "s_mov_b32 s67, 8320\n"
    "s_setprio 0\n"
    "s_branch L_ET1_%=\n"
    "L_EG0_%=:\n"
    "s_waitcnt lgkmcnt(0)\n"
    "s_barrier\n"
    "s_add_u32 s64, s64, 1\n"
    "L_G0_%=:\n"
    "s_waitcnt vmcnt(3)\n"
    "v_and_b32 v0, s43, v48\n"
    "v_lshrrev_b32 v4, 16, v48\n"
    "ds_read_b128 v[0:3], v0\n"
    "ds_read_b128 v[4:7], v4\n"
    "v_and_b32 v8, s43, v49\n"
    "v_lshrrev_b32 v12, 16, v49\n"
    "ds_read_b128 v[8:11], v8\n"
    "ds_read_b128 v[12:15], v12\n"
    "s_cmp_eq_u32 s64, 2\n"
    "s_cbranch_scc1 L_IM0_%=\n"
    "s_mov_b32 m0, s61\n"
    "s_add_u32 s65, s57, s59\n"
    "buffer_load_dwordx4 v58, s[44:47], s65 offen lds\n"
    "L_IM0r_%=:\n"
    "v_and_b32 v16, s43, v50\n"
    "v_lshrrev_b32 v20, 16, v50\n"
    "ds_read_b128 v[16:19], v16\n"
    "ds_read_b128 v[20:23], v20\n"
    "s_add_u32 s56, s56, 32768\n"
    "s_mov_b32 s57, s58\n"
    "s_add_u32 s58, s58, 524288\n"
    "s_add_u32 s61, s61, 8320\n"
    "s_add_u32 s62, s62, 1\n"
    "v_and_b32 v24, s43, v51\n"
    "v_lshrrev_b32 v28, 16, v51\n"
    "ds_read_b128 v[24:27], v24\n"
    "ds_read_b128 v[28:31], v28\n"
    "s_cmp_eq_u32 s62, 2\n"
    "s_cselect_b32 s58, 12582912, s58\n"
    "s_cmp_eq_u32 s62, 3\n"
    "s_cselect_b32 s58, 1073741824, s58\n"
    "s_cselect_b32 s61, s53, s61\n"
    "s_waitcnt vmcnt(3)\n"
    "v_and_b32 v44, s43, v52\n"
    "v_lshrrev_b32 v52, 16, v52\n"
    "v_and_b32 v45, s43, v53\n"
    "v_lshrrev_b32 v53, 16, v53\n"
    "s_waitcnt lgkmcnt(6)\n"
    "s_setprio 2\n"
    "v_smfmac_f32_16x16x64_bf16 v[64:67], v[60:63], v[0:7], v59\n"
    "s_waitcnt lgkmcnt(4)\n"
    "v_smfmac_f32_16x16x64_bf16 v[68:71], v[60:63], v[8:15], v59\n"
    "s_setprio 0\n"
    "ds_read_b128 v[0:3], v44\n"
    "ds_read_b128 v[4:7], v52\n"
    "ds_read_b128 v[8:11], v45\n"
    "ds_read_b128 v[12:15], v53\n"
    "v_and_b32 v46, s43, v54\n"
    "v_lshrrev_b32 v54, 16, v54\n"
    "v_and_b32 v47, s43, v55\n"
    "v_lshrrev_b32 v55, 16, v55\n"
    "s_waitcnt lgkmcnt(6)\n"
    "s_setprio 2\n"
    "v_smfmac_f32_16x16x64_bf16 v[72:75], v[60:63], v[16:23], v59\n"
    "s_waitcnt lgkmcnt(4)\n"
    "v_smfmac_f32_16x16x64_bf16 v[76:79], v[60:63], v[24:31], v59\n"
    "s_setprio 0\n"
    "ds_read_b128 v[16:19], v46\n"
    "ds_read_b128 v[20:23], v54\n"
    "ds_read_b128 v[24:27], v47\n"
    "ds_read_b128 v[28:31], v55\n"
    "s_waitcnt vmcnt(1)\n"
    "v_and_b32 v44, s43, v32\n"
    "v_lshrrev_b32 v32, 16, v32\n"
    "v_and_b32 v45, s43, v33\n"
    "v_lshrrev_b32 v33, 16, v33\n"
    "s_waitcnt lgkmcnt(6)\n"
    "s_setprio 2\n"
    "v_smfmac_f32_16x16x64_bf16 v[80:83], v[60:63], v[0:7], v59\n"
    "s_waitcnt lgkmcnt(4)\n"
    "v_smfmac_f32_16x16x64_bf16 v[84:87], v[60:63], v[8:15], v59\n"
    "s_setprio 0\n"
    "ds_read_b128 v[0:3], v44\n"
    "ds_read_b128 v[4:7], v32\n"
    "ds_read_b128 v[8:11], v45\n"
    "ds_read_b128 v[12:15], v33\n"
    "s_cmp_eq_u32 s64, 2\n"
    "s_cbranch_scc1 L_CF_%=\n"
    "v_mov_b32 v40, -1\n"
    "v_mov_b32 v41, -1\n"
    "v_mov_b32 v42, -1\n"
    "v_mov_b32 v43, -1\n"
    "buffer_store_dwordx4 v[40:43], v57, s[48:51], s31 offen sc1\n"
    "s_add_u32 s31, s31, 1024\n"
    "L_C1r_%=:\n"
    "v_and_b32 v46, s43, v34\n"
    "v_lshrrev_b32 v34, 16, v34\n"
    "v_and_b32 v47, s43, v35\n"
    "v_lshrrev_b32 v35, 16, v35\n"
    "s_waitcnt lgkmcnt(6)\n"
    "s_setprio 2\n"
    "v_smfmac_f32_16x16x64_bf16 v[88:91], v[60:63], v[16:23], v59\n"
    "s_waitcnt lgkmcnt(4)\n"
    "v_smfmac_f32_16x16x64_bf16 v[92:95], v[60:63], v[24:31], v59\n"
    "s_setprio 0\n"
    "ds_read_b128 v[16:19], v46\n"
    "ds_read_b128 v[20:23], v34\n"
    "ds_read_b128 v[24:27], v47\n"
    "ds_read_b128 v[28:31], v35\n"
    "v_and_b32 v44, s43, v36\n"
    "v_lshrrev_b32 v36, 16, v36\n"
    "v_and_b32 v45, s43, v37\n"
    "v_lshrrev_b32 v37, 16, v37\n"
    "s_waitcnt lgkmcnt(6)\n"
    "s_setprio 2\n"
    "v_smfmac_f32_16x16x64_bf16 v[96:99], v[60:63], v[0:7], v59\n"
    "s_waitcnt lgkmcnt(4)\n"
    "v_smfmac_f32_16x16x64_bf16 v[100:103], v[60:63], v[8:15], v59\n"
    "s_setprio 0\n"
    "ds_read_b128 v[0:3], v44\n"
    "ds_read_b128 v[4:7], v36\n"
    "ds_read_b128 v[8:11], v45\n"
    "ds_read_b128 v[12:15], v37\n"
    "s_cmp_eq_u32 s64, 2\n"
    "s_cbranch_scc1 L_PK_%=\n"
    "L_D0r_%=:\n"
    "v_and_b32 v46, s43, v38\n"
    "v_lshrrev_b32 v38, 16, v38\n"
    "v_and_b32 v47, s43, v39\n"
    "v_lshrrev_b32 v39, 16, v39\n"
    "s_waitcnt lgkmcnt(6)\n"
    "s_setprio 2\n"
    "v_smfmac_f32_16x16x64_bf16 v[104:107], v[60:63], v[16:23], v59\n"
    "s_waitcnt lgkmcnt(4)\n"
    "v_smfmac_f32_16x16x64_bf16 v[108:111], v[60:63], v[24:31], v59\n"
    "s_setprio 0\n"
    "ds_read_b128 v[16:19], v46\n"
    "ds_read_b128 v[20:23], v38\n"
    "ds_read_b128 v[24:27], v47\n"
    "ds_read_b128 v[28:31], v39\n"
    "s_waitcnt lgkmcnt(6)\n"
    "s_setprio 2\n"
    "v_smfmac_f32_16x16x64_bf16 v[112:115], v[60:63], v[0:7], v59\n"
    "s_waitcnt lgkmcnt(4)\n"
    "v_smfmac_f32_16x16x64_bf16 v[116:119], v[60:63], v[8:15], v59\n"
    "s_setprio 0\n"
    "s_waitcnt lgkmcnt(2)\n"
    "s_setprio 2\n"
    "v_smfmac_f32_16x16x64_bf16 v[120:123], v[60:63], v[16:23], v59\n"
    "s_waitcnt lgkmcnt(0)\n"
    "v_smfmac_f32_16x16x64_bf16 v[124:127], v[60:63], v[24:31], v59\n"
    "s_setprio 0\n"
    "s_cmp_eq_u32 s64, 2\n"
    "s_cbranch_vccz L_T1_%=\n"
    "s_cbranch_scc0 L_ET0_%=\n"
    "s_branch L_GLA_%=\n"
    "L_ET1_%=:\n"
    "s_waitcnt lgkmcnt(0)\n"
    "s_barrier\n"
    "s_add_u32 s64, s64, 1\n"
    "L_T1_%=:\n"
    "s_setprio 1\n"
    "ds_read_b64_tr_b16 v[0:1], v56\n"
    "ds_read_b64_tr_b16 v[2:3], v56 offset:64\n"
    "ds_read_b64_tr_b16 v[4:5], v56 offset:512\n"
    "ds_read_b64_tr_b16 v[6:7], v56 offset:576\n"
    "ds_read_b64_tr_b16 v[8:9], v56 offset:1024\n"
    "ds_read_b64_tr_b16 v[10:11], v56 offset:1088\n"
    "ds_read_b64_tr_b16 v[12:13], v56 offset:1536\n"
    "ds_read_b64_tr_b16 v[14:15], v56 offset:1600\n"
    "ds_read_b64_tr_b16 v[16:17], v56 offset:8\n"
    "ds_read_b64_tr_b16 v[18:19], v56 offset:72\n"
    "ds_read_b64_tr_b16 v[20:21], v56 offset:520\n"
    "ds_read_b64_tr_b16 v[22:23], v56 offset:584\n"
    "ds_read_b64_tr_b16 v[24:25], v56 offset:1032\n"
    "ds_read_b64_tr_b16 v[26:27], v56 offset:1096\n"
    "ds_read_b64_tr_b16 v[28:29], v56 offset:1544\n"
    "ds_read_b64_tr_b16 v[30:31], v56 offset:1608\n"
    "buffer_load_dwordx4 v[48:51], v57, s[48:51], s56 offen sc1\n"
    "buffer_load_dwordx4 v[52:55], v57, s[48:51], s56 offen offset:1024 sc1\n"
    "s_waitcnt lgkmcnt(14)\n"
    "v_mfma_f32_16x16x32_bf16 v[32:35], v[128:131], v[0:3], 0\n"
    "s_waitcnt lgkmcnt(12)\n"
    "v_mfma_f32_16x16x32_bf16 v[32:35], v[132:135], v[4:7], v[32:35]\n"
    "s_mov_b32 m0, s55\n"
    "s_waitcnt lgkmcnt(10)\n"
    "v_mfma_f32_16x16x32_bf16 v[32:35], v[136:139], v[8:11], v[32:35]\n"
    "s_add_u32 s63, s63, 1\n"
    "s_cmp_eq_u32 s63, 3\n"
    "s_cselect_b32 s65, -16640, s67\n"
    "s_cselect_b32 s63, 0, s63\n"
    "s_waitcnt lgkmcnt(8)\n"
    "v_mfma_f32_16x16x32_bf16 v[32:35], v[140:143], v[12:15], v[32:35]\n"
    "v_add_u32 v56, s65, v56\n"
    "s_waitcnt lgkmcnt(6)\n"
    "v_mfma_f32_16x16x32_bf16 v[36:39], v[128:131], v[16:19], 0\n"
    "s_waitcnt lgkmcnt(4)\n"
    "v_mfma_f32_16x16x32_bf16 v[36:39], v[132:135], v[20:23], v[36:39]\n"
    "s_waitcnt lgkmcnt(2)\n"
    "v_mfma_f32_16x16x32_bf16 v[36:39], v[136:139], v[24:27], v[36:39]\n"
    "s_waitcnt lgkmcnt(0)\n"
    "v_mfma_f32_16x16x32_bf16 v[36:39], v[140:143], v[28:31], v[36:39]\n"
    "v_mfma_f32_16x16x32_bf16 v[40:43], v[144:147], v[0:3], 0\n"
    "v_mfma_f32_16x16x32_bf16 v[40:43], v[148:151], v[4:7], v[40:43]\n"
    "v_mfma_f32_16x16x32_bf16 v[40:43], v[152:155], v[8:11], v[40:43]\n"
    "v_mfma_f32_16x16x32_bf16 v[40:43], v[156:159], v[12:15], v[40:43]\n"
    "v_mfma_f32_16x16x32_bf16 v[44:47], v[144:147], v[16:19], 0\n"
    "s_nop 2\n"
    "v_cvt_pk_bf16_f32 v36, v32, v36\n"
    "v_mfma_f32_16x16x32_bf16 v[44:47], v[148:151], v[20:23], v[44:47]\n"
    "v_cvt_pk_bf16_f32 v37, v33, v37\n"
    "v_mfma_f32_16x16x32_bf16 v[44:47], v[152:155], v[24:27], v[44:47]\n"
    "v_cvt_pk_bf16_f32 v38, v34, v38\n"
    "v_mfma_f32_16x16x32_bf16 v[44:47], v[156:159], v[28:31], v[44:47]\n"
    "v_cvt_pk_bf16_f32 v39, v35, v39\n"
    "v_mfma_f32_16x16x32_bf16 v[32:35], v[160:163], v[0:3], 0\n"
    "ds_write_addtid_b32 v36 offset:57348\n"
    "v_mfma_f32_16x16x32_bf16 v[32:35], v[164:167], v[4:7], v[32:35]\n"
    "ds_write_addtid_b32 v37 offset:57604\n"
    "v_mfma_f32_16x16x32_bf16 v[32:35], v[168:171], v[8:11], v[32:35]\n"
    "ds_write_addtid_b32 v38 offset:57860\n"
    "v_mfma_f32_16x16x32_bf16 v[32:35], v[172:175], v[12:15], v[32:35]\n"
    "ds_write_addtid_b32 v39 offset:58116\n"
    "v_mfma_f32_16x16x32_bf16 v[36:39], v[160:163], v[16:19], 0\n"
    "v_cvt_pk_bf16_f32 v44, v40, v44\n"
    "v_mfma_f32_16x16x32_bf16 v[36:39], v[164:167], v[20:23], v[36:39]\n"
    "v_cvt_pk_bf16_f32 v45, v41, v45\n"
    "v_mfma_f32_16x16x32_bf16 v[36:39], v[168:171], v[24:27], v[36:39]\n"
    "v_cvt_pk_bf16_f32 v46, v42, v46\n"
    "v_mfma_f32_16x16x32_bf16 v[36:39], v[172:175], v[28:31], v[36:39]\n"
    "v_cvt_pk_bf16_f32 v47, v43, v47\n"
    "v_mfma_f32_16x16x32_bf16 v[40:43], v[176:179], v[0:3], 0\n"
    "ds_write_addtid_b32 v44 offset:58372\n"
    "v_mfma_f32_16x16x32_bf16 v[40:43], v[180:183], v[4:7], v[40:43]\n"
    "ds_write_addtid_b32 v45 offset:58628\n"
    "v_mfma_f32_16x16x32_bf16 v[40:43], v[184:187], v[8:11], v[40:43]\n"
    "ds_write_addtid_b32 v46 offset:58884\n"
    "v_mfma_f32_16x16x32_bf16 v[40:43], v[188:191], v[12:15], v[40:43]\n"
    "ds_write_addtid_b32 v47 offset:59140\n"
    "v_mfma_f32_16x16x32_bf16 v[44:47], v[176:179], v[16:19], 0\n"
    "v_cvt_pk_bf16_f32 v36, v32, v36\n"
    "v_mfma_f32_16x16x32_bf16 v[44:47], v[180:183], v[20:23], v[44:47]\n"
    "v_cvt_pk_bf16_f32 v37, v33, v37\n"
    "v_mfma_f32_16x16x32_bf16 v[44:47], v[184:187], v[24:27], v[44:47]\n"
    "v_cvt_pk_bf16_f32 v38, v34, v38\n"
    "v_mfma_f32_16x16x32_bf16 v[44:47], v[188:191], v[28:31], v[44:47]\n"
    "v_cvt_pk_bf16_f32 v39, v35, v39\n"
    "v_mfma_f32_16x16x32_bf16 v[32:35], v[192:195], v[0:3], 0\n"
    "ds_write_addtid_b32 v36 offset:59396\n"
    "v_mfma_f32_16x16x32_bf16 v[32:35], v[196:199], v[4:7], v[32:35]\n"
    "ds_write_addtid_b32 v37 offset:59652\n"
    "v_mfma_f32_16x16x32_bf16 v[32:35], v[200:203], v[8:11], v[32:35]\n"
    "ds_write_addtid_b32 v38 offset:59908\n"
    "v_mfma_f32_16x16x32_bf16 v[32:35], v[204:207], v[12:15], v[32:35]\n"
    "ds_write_addtid_b32 v39 offset:60164\n"
    "v_mfma_f32_16x16x32_bf16 v[36:39], v[192:195], v[16:19], 0\n"
    "v_cvt_pk_bf16_f32 v44, v40, v44\n"
    "v_mfma_f32_16x16x32_bf16 v[36:39], v[196:199], v[20:23], v[36:39]\n"
    "v_cvt_pk_bf16_f32 v45, v41, v45\n"
    "v_mfma_f32_16x16x32_bf16 v[36:39], v[200:203], v[24:27], v[36:39]\n"
    "v_cvt_pk_bf16_f32 v46, v42, v46\n"
    "v_mfma_f32_16x16x32_bf16 v[36:39], v[204:207], v[28:31], v[36:39]\n"
    "v_cvt_pk_bf16_f32 v47, v43, v47\n"
    "v_mfma_f32_16x16x32_bf16 v[40:43], v[208:211], v[0:3], 0\n"
    "ds_write_addtid_b32 v44 offset:60420\n"
    "v_mfma_f32_16x16x32_bf16 v[40:43], v[212:215], v[4:7], v[40:43]\n"
    "ds_write_addtid_b32 v45 offset:60676\n"
    "v_mfma_f32_16x16x32_bf16 v[40:43], v[216:219], v[8:11], v[40:43]\n"
    "ds_write_addtid_b32 v46 offset:60932\n"
    "v_mfma_f32_16x16x32_bf16 v[40:43], v[220:223], v[12:15], v[40:43]\n"
    "ds_write_addtid_b32 v47 offset:61188\n"
    "v_mfma_f32_16x16x32_bf16 v[44:47], v[208:211], v[16:19], 0\n"
    "v_cvt_pk_bf16_f32 v36, v32, v36\n"
    "v_mfma_f32_16x16x32_bf16 v[44:47], v[212:215], v[20:23], v[44:47]\n"
    "v_cvt_pk_bf16_f32 v37, v33, v37\n"
    "v_mfma_f32_16x16x32_bf16 v[44:47], v[216:219], v[24:27], v[44:47]\n"
    "v_cvt_pk_bf16_f32 v38, v34, v38\n"
    "v_mfma_f32_16x16x32_bf16 v[44:47], v[220:223], v[28:31], v[44:47]\n"
    "v_cvt_pk_bf16_f32 v39, v35, v39\n"
    "v_mfma_f32_16x16x32_bf16 v[32:35], v[224:227], v[0:3], 0\n"
    "ds_write_addtid_b32 v36 offset:61444\n"
    "v_mfma_f32_16x16x32_bf16 v[32:35], v[228:231], v[4:7], v[32:35]\n"
    "ds_write_addtid_b32 v37 offset:61700\n"
    "v_mfma_f32_16x16x32_bf16 v[32:35], v[232:235], v[8:11], v[32:35]\n"
    "ds_write_addtid_b32 v38 offset:61956\n"
    "v_mfma_f32_16x16x32_bf16 v[32:35], v[236:239], v[12:15], v[32:35]\n"
    "ds_write_addtid_b32 v39 offset:62212\n"
    "v_mfma_f32_16x16x32_bf16 v[36:39], v[224:227], v[16:19], 0\n"
    "v_cvt_pk_bf16_f32 v44, v40, v44\n"
    "v_mfma_f32_16x16x32_bf16 v[36:39], v[228:231], v[20:23], v[36:39]\n"
    "v_cvt_pk_bf16_f32 v45, v41, v45\n"
    "v_mfma_f32_16x16x32_bf16 v[36:39], v[232:235], v[24:27], v[36:39]\n"
    "v_cvt_pk_bf16_f32 v46, v42, v46\n"
    "v_mfma_f32_16x16x32_bf16 v[36:39], v[236:239], v[28:31], v[36:39]\n"
    "v_cvt_pk_bf16_f32 v47, v43, v47\n"
    "v_mfma_f32_16x16x32_bf16 v[40:43], v[240:243], v[0:3], 0\n"
    "ds_write_addtid_b32 v44 offset:62468\n"
    "v_mfma_f32_16x16x32_bf16 v[40:43], v[244:247], v[4:7], v[40:43]\n"
    "ds_write_addtid_b32 v45 offset:62724\n"
    "v_mfma_f32_16x16x32_bf16 v[40:43], v[248:251], v[8:11], v[40:43]\n"
    "ds_write_addtid_b32 v46 offset:62980\n"
    "v_mfma_f32_16x16x32_bf16 v[40:43], v[252:255], v[12:15], v[40:43]\n"
    "ds_write_addtid_b32 v47 offset:63236\n"
    "v_mfma_f32_16x16x32_bf16 v[44:47], v[240:243], v[16:19], 0\n"
    "v_cvt_pk_bf16_f32 v36, v32, v36\n"
    "v_mfma_f32_16x16x32_bf16 v[44:47], v[244:247], v[20:23], v[44:47]\n"
    "v_cvt_pk_bf16_f32 v37, v33, v37\n"
    "v_mfma_f32_16x16x32_bf16 v[44:47], v[248:251], v[24:27], v[44:47]\n"
    "v_cvt_pk_bf16_f32 v38, v34, v38\n"
    "v_mfma_f32_16x16x32_bf16 v[44:47], v[252:255], v[28:31], v[44:47]\n"
    "v_cvt_pk_bf16_f32 v39, v35, v39\n"
    "buffer_load_dwordx4 v[32:35], v57, s[48:51], s56 offen offset:2048 sc1\n"
    "ds_write_addtid_b32 v36 offset:63492\n"
    "ds_write_addtid_b32 v37 offset:63748\n"
    "ds_write_addtid_b32 v38 offset:64004\n"
    "ds_write_addtid_b32 v39 offset:64260\n"
    "buffer_load_dwordx4 v[36:39], v57, s[48:51], s56 offen offset:3072 sc1\n"
    "s_nop 0\n"
    "v_cvt_pk_bf16_f32 v44, v40, v44\n"
    "v_cvt_pk_bf16_f32 v45, v41, v45\n"
    "v_cvt_pk_bf16_f32 v46, v42, v46\n"
    "v_cvt_pk_bf16_f32 v47, v43, v47\n"
    "ds_write_addtid_b32 v44 offset:64516\n"
    "ds_write_addtid_b32 v45 offset:64772\n"
    "ds_write_addtid_b32 v46 offset:65028\n"
    "ds_write_addtid_b32 v47 offset:65284\n"
    "s_setprio 0\n"
    "s_cbranch_vccnz L_G0_%=\n"
    "s_cmp_eq_u32 s64, 2\n"
    "s_cbranch_scc1 L_GLB_%=\n"
    "s_waitcnt lgkmcnt(0)\n"
    "s_barrier\n"
    "L_G1_%=:\n"
    "s_waitcnt vmcnt(3)\n"
    "v_and_b32 v0, s43, v48\n"
    "v_lshrrev_b32 v4, 16, v48\n"
    "ds_read_b128 v[0:3], v0 offset:65520\n"
    "ds_read_b128 v[4:7], v4 offset:65520\n"
    "v_and_b32 v8, s43, v49\n"
    "v_lshrrev_b32 v12, 16, v49\n"
    "ds_read_b128 v[8:11], v8 offset:65520\n"
    "ds_read_b128 v[12:15], v12 offset:65520\n"
    "s_mov_b32 m0, s61\n"
    "s_add_u32 s65, s57, s59\n"
    "buffer_load_dwordx4 v58, s[44:47], s65 offen lds\n"
    "v_and_b32 v16, s43, v50\n"
    "v_lshrrev_b32 v20, 16, v50\n"
    "ds_read_b128 v[16:19], v16 offset:65520\n"
    "ds_read_b128 v[20:23], v20 offset:65520\n"
    "s_add_u32 s56, s56, 32768\n"
    "s_mov_b32 s57, s58\n"
    "s_add_u32 s58, s58, 524288\n"
    "s_add_u32 s61, s61, 8320\n"
    "s_add_u32 s62, s62, 1\n"
    "v_and_b32 v24, s43, v51\n"
    "v_lshrrev_b32 v28, 16, v51\n"
    "ds_read_b128 v[24:27], v24 offset:65520\n"
    "ds_read_b128 v[28:31], v28 offset:65520\n"
    "s_cmp_eq_u32 s62, 2\n"
    "s_cselect_b32 s58, 12582912, s58\n"
    "s_cmp_eq_u32 s62, 3\n"
    "s_cselect_b32 s58, 1073741824, s58\n"
    "s_cselect_b32 s61, s53, s61\n"
    "s_waitcnt vmcnt(3)\n"
    "v_and_b32 v44, s43, v52\n"
    "v_lshrrev_b32 v52, 16, v52\n"
    "v_and_b32 v45, s43, v53\n"
    "v_lshrrev_b32 v53, 16, v53\n"
    "s_waitcnt lgkmcnt(6)\n"
    "s_setprio 2\n"
    "v_smfmac_f32_16x16x64_bf16 v[64:67], v[60:63], v[0:7], v59\n"
    "s_waitcnt lgkmcnt(4)\n"
    "v_smfmac_f32_16x16x64_bf16 v[68:71], v[60:63], v[8:15], v59\n"
    "s_setprio 0\n"
    "ds_read_b128 v[0:3], v44 offset:65520\n"
    "ds_read_b128 v[4:7], v52 offset:65520\n"
    "ds_read_b128 v[8:11], v45 offset:65520\n"
    "ds_read_b128 v[12:15], v53 offset:65520\n"
    "v_and_b32 v46, s43, v54\n"
    "v_lshrrev_b32 v54, 16, v54\n"
    "v_and_b32 v47, s43, v55\n"
    "v_lshrrev_b32 v55, 16, v55\n"
    "s_waitcnt lgkmcnt(6)\n"
    "s_setprio 2\n"
    "v_smfmac_f32_16x16x64_bf16 v[72:75], v[60:63], v[16:23], v59\n"
    "s_waitcnt lgkmcnt(4)\n"
    "v_smfmac_f32_16x16x64_bf16 v[76:79], v[60:63], v[24:31], v59\n"
    "s_setprio 0\n"
    "ds_read_b128 v[16:19], v46 offset:65520\n"
    "ds_read_b128 v[20:23], v54 offset:65520\n"
    "ds_read_b128 v[24:27], v47 offset:65520\n"
    "ds_read_b128 v[28:31], v55 offset:65520\n"
    "s_waitcnt vmcnt(1)\n"
    "v_and_b32 v44, s43, v32\n"
    "v_lshrrev_b32 v32, 16, v32\n"
    "v_and_b32 v45, s43, v33\n"
    "v_lshrrev_b32 v33, 16, v33\n"
    "s_waitcnt lgkmcnt(6)\n"
    "s_setprio 2\n"
    "v_smfmac_f32_16x16x64_bf16 v[80:83], v[60:63], v[0:7], v59\n"
    "s_waitcnt lgkmcnt(4)\n"
    "v_smfmac_f32_16x16x64_bf16 v[84:87], v[60:63], v[8:15], v59\n"
    "s_setprio 0\n"
    "ds_read_b128 v[0:3], v44 offset:65520\n"
    "ds_read_b128 v[4:7], v32 offset:65520\n"
    "ds_read_b128 v[8:11], v45 offset:65520\n"
    "ds_read_b128 v[12:15], v33 offset:65520\n"
    "v_mov_b32 v40, -1\n"
    "v_mov_b32 v41, -1\n"
    "v_mov_b32 v42, -1\n"
    "v_mov_b32 v43, -1\n"
    "buffer_store_dwordx4 v[40:43], v57, s[48:51], s31 offen sc1\n"
    "s_add_u32 s31, s31, 1024\n"
    "v_and_b32 v46, s43, v34\n"
    "v_lshrrev_b32 v34, 16, v34\n"
    "v_and_b32 v47, s43, v35\n"
    "v_lshrrev_b32 v35, 16, v35\n"
    "s_waitcnt lgkmcnt(6)\n"
    "s_setprio 2\n"
    "v_smfmac_f32_16x16x64_bf16 v[88:91], v[60:63], v[16:23], v59\n"
    "s_waitcnt lgkmcnt(4)\n"
    "v_smfmac_f32_16x16x64_bf16 v[92:95], v[60:63], v[24:31], v59\n"
    "s_setprio 0\n"
    "ds_read_b128 v[16:19], v46 offset:65520\n"
    "ds_read_b128 v[20:23], v34 offset:65520\n"
    "ds_read_b128 v[24:27], v47 offset:65520\n"
    "ds_read_b128 v[28:31], v35 offset:65520\n"
    "v_and_b32 v44, s43, v36\n"
    "v_lshrrev_b32 v36, 16, v36\n"
    "v_and_b32 v45, s43, v37\n"
    "v_lshrrev_b32 v37, 16, v37\n"
    "s_waitcnt lgkmcnt(6)\n"
    "s_setprio 2\n"
    "v_smfmac_f32_16x16x64_bf16 v[96:99], v[60:63], v[0:7], v59\n"
    "s_waitcnt lgkmcnt(4)\n"
    "v_smfmac_f32_16x16x64_bf16 v[100:103], v[60:63], v[8:15], v59\n"
    "s_setprio 0\n"
    "ds_read_b128 v[0:3], v44 offset:65520\n"
    "ds_read_b128 v[4:7], v36 offset:65520\n"
    "ds_read_b128 v[8:11], v45 offset:65520\n"
    "ds_read_b128 v[12:15], v37 offset:65520\n"
    "v_and_b32 v46, s43, v38\n"
    "v_lshrrev_b32 v38, 16, v38\n"
    "v_and_b32 v47, s43, v39\n"
    "v_lshrrev_b32 v39, 16, v39\n"
    "s_waitcnt lgkmcnt(6)\n"
    "s_setprio 2\n"
    "v_smfmac_f32_16x16x64_bf16 v[104:107], v[60:63], v[16:23], v59\n"
    "s_waitcnt lgkmcnt(4)\n"
    "v_smfmac_f32_16x16x64_bf16 v[108:111], v[60:63], v[24:31], v59\n"
    "s_setprio 0\n"
    "ds_read_b128 v[16:19], v46 offset:65520\n"
    "ds_read_b128 v[20:23], v38 offset:65520\n"
    "s_cmp_eq_u32 s64, 0\n"
    "s_cbranch_scc1 L_D14r_%=\n"
    "buffer_store_dwordx4 v[40:43], v57, s[48:51], s31 offen sc1\n"
    "s_add_u32 s31, s31, 1024\n"
    "L_D14r_%=:\n"
    "ds_read_b128 v[24:27], v47 offset:65520\n"
    "ds_read_b128 v[28:31], v39 offset:65520\n"
    "s_waitcnt lgkmcnt(6)\n"
    "s_setprio 2\n"
    "v_smfmac_f32_16x16x64_bf16 v[112:115], v[60:63], v[0:7], v59\n"
    "s_waitcnt lgkmcnt(4)\n"
    "v_smfmac_f32_16x16x64_bf16 v[116:119], v[60:63], v[8:15], v59\n"
    "s_setprio 0\n"
    "s_waitcnt lgkmcnt(2)\n"
    "s_setprio 2\n"
    "v_smfmac_f32_16x16x64_bf16 v[120:123], v[60:63], v[16:23], v59\n"
    "s_waitcnt lgkmcnt(0)\n"
    "v_smfmac_f32_16x16x64_bf16 v[124:127], v[60:63], v[24:31], v59\n"
    "s_setprio 0\n"
    "s_cbranch_vccz L_T0_%=\n"
    "s_branch L_ET1_%=\n"
    "L_ET0_%=:\n"
    "s_waitcnt lgkmcnt(0)\n"
    "s_barrier\n"
    "L_T0_%=:\n"
    "s_setprio 1\n"
    "ds_read_b64_tr_b16 v[0:1], v56\n"
    "ds_read_b64_tr_b16 v[2:3], v56 offset:64\n"
    "ds_read_b64_tr_b16 v[4:5], v56 offset:512\n"
    "ds_read_b64_tr_b16 v[6:7], v56 offset:576\n"
    "ds_read_b64_tr_b16 v[8:9], v56 offset:1024\n"
    "ds_read_b64_tr_b16 v[10:11], v56 offset:1088\n"
    "ds_read_b64_tr_b16 v[12:13], v56 offset:1536\n"
    "ds_read_b64_tr_b16 v[14:15], v56 offset:1600\n"
    "ds_read_b64_tr_b16 v[16:17], v56 offset:8\n"
    "ds_read_b64_tr_b16 v[18:19], v56 offset:72\n"
    "ds_read_b64_tr_b16 v[20:21], v56 offset:520\n"
    "ds_read_b64_tr_b16 v[22:23], v56 offset:584\n"
    "ds_read_b64_tr_b16 v[24:25], v56 offset:1032\n"
    "ds_read_b64_tr_b16 v[26:27], v56 offset:1096\n"
    "ds_read_b64_tr_b16 v[28:29], v56 offset:1544\n"
    "ds_read_b64_tr_b16 v[30:31], v56 offset:1608\n"
    "buffer_load_dwordx4 v[48:51], v57, s[48:51], s56 offen sc1\n"
    "buffer_load_dwordx4 v[52:55], v57, s[48:51], s56 offen offset:1024 sc1\n"
    "s_waitcnt lgkmcnt(14)\n"
    "v_mfma_f32_16x16x32_bf16 v[32:35], v[128:131], v[0:3], 0\n"
    "s_waitcnt lgkmcnt(12)\n"
    "v_mfma_f32_16x16x32_bf16 v[32:35], v[132:135], v[4:7], v[32:35]\n"
    "s_mov_b32 m0, s54\n"
    "s_waitcnt lgkmcnt(10)\n"
    "v_mfma_f32_16x16x32_bf16 v[32:35], v[136:139], v[8:11], v[32:35]\n"
    "s_add_u32 s63, s63, 1\n"
    "s_cmp_eq_u32 s63, 3\n"
    "s_cselect_b32 s65, -16640, s67\n"
    "s_cselect_b32 s63, 0, s63\n"
    "s_waitcnt lgkmcnt(8)\n"
    "v_mfma_f32_16x16x32_bf16 v[32:35], v[140:143], v[12:15], v[32:35]\n"
    "v_add_u32 v56, s65, v56\n"
    "s_waitcnt lgkmcnt(6)\n"
    "v_mfma_f32_16x16x32_bf16 v[36:39], v[128:131], v[16:19], 0\n"
    "s_waitcnt lgkmcnt(4)\n"
    "v_mfma_f32_16x16x32_bf16 v[36:39], v[132:135], v[20:23], v[36:39]\n"
    "s_waitcnt lgkmcnt(2)\n"
    "v_mfma_f32_16x16x32_bf16 v[36:39], v[136:139], v[24:27], v[36:39]\n"
    "s_waitcnt lgkmcnt(0)\n"
    "v_mfma_f32_16x16x32_bf16 v[36:39], v[140:143], v[28:31], v[36:39]\n"
    "v_mfma_f32_16x16x32_bf16 v[40:43], v[144:147], v[0:3], 0\n"
    "v_mfma_f32_16x16x32_bf16 v[40:43], v[148:151], v[4:7], v[40:43]\n"
    "v_mfma_f32_16x16x32_bf16 v[40:43], v[152:155], v[8:11], v[40:43]\n"
    "v_mfma_f32_16x16x32_bf16 v[40:43], v[156:159], v[12:15], v[40:43]\n"
    "v_mfma_f32_16x16x32_bf16 v[44:47], v[144:147], v[16:19], 0\n"
    "s_nop 2\n"
    "v_cvt_pk_bf16_f32 v36, v32, v36\n"
    "v_mfma_f32_16x16x32_bf16 v[44:47], v[148:151], v[20:23], v[44:47]\n"
    "v_cvt_pk_bf16_f32 v37, v33, v37\n"
    "v_mfma_f32_16x16x32_bf16 v[44:47], v[152:155], v[24:27], v[44:47]\n"
    "v_cvt_pk_bf16_f32 v38, v34, v38\n"
    "v_mfma_f32_16x16x32_bf16 v[44:47], v[156:159], v[28:31], v[44:47]\n"
    "v_cvt_pk_bf16_f32 v39, v35, v39\n"
    "v_mfma_f32_16x16x32_bf16 v[32:35], v[160:163], v[0:3], 0\n"
    "ds_write_addtid_b32 v36 offset:0\n"
    "v_mfma_f32_16x16x32_bf16 v[32:35], v[164:167], v[4:7], v[32:35]\n"
    "ds_write_addtid_b32 v37 offset:256\n"
    "v_mfma_f32_16x16x32_bf16 v[32:35], v[168:171], v[8:11], v[32:35]\n"
    "ds_write_addtid_b32 v38 offset:512\n"
    "v_mfma_f32_16x16x32_bf16 v[32:35], v[172:175], v[12:15], v[32:35]\n"
    "ds_write_addtid_b32 v39 offset:768\n"
    "v_mfma_f32_16x16x32_bf16 v[36:39], v[160:163], v[16:19], 0\n"
    "v_cvt_pk_bf16_f32 v44, v40, v44\n"
    "v_mfma_f32_16x16x32_bf16 v[36:39], v[164:167], v[20:23], v[36:39]\n"
    "v_cvt_pk_bf16_f32 v45, v41, v45\n"
    "v_mfma_f32_16x16x32_bf16 v[36:39], v[168:171], v[24:27], v[36:39]\n"
    "v_cvt_pk_bf16_f32 v46, v42, v46\n"
    "v_mfma_f32_16x16x32_bf16 v[36:39], v[172:175], v[28:31], v[36:39]\n"
    "v_cvt_pk_bf16_f32 v47, v43, v47\n"
    "v_mfma_f32_16x16x32_bf16 v[40:43], v[176:179], v[0:3], 0\n"
    "ds_write_addtid_b32 v44 offset:1024\n"
    "v_mfma_f32_16x16x32_bf16 v[40:43], v[180:183], v[4:7], v[40:43]\n"
    "ds_write_addtid_b32 v45 offset:1280\n"
    "v_mfma_f32_16x16x32_bf16 v[40:43], v[184:187], v[8:11], v[40:43]\n"
    "ds_write_addtid_b32 v46 offset:1536\n"
    "v_mfma_f32_16x16x32_bf16 v[40:43], v[188:191], v[12:15], v[40:43]\n"
    "ds_write_addtid_b32 v47 offset:1792\n"
    "v_mfma_f32_16x16x32_bf16 v[44:47], v[176:179], v[16:19], 0\n"
    "v_cvt_pk_bf16_f32 v36, v32, v36\n"
    "v_mfma_f32_16x16x32_bf16 v[44:47], v[180:183], v[20:23], v[44:47]\n"
    "v_cvt_pk_bf16_f32 v37, v33, v37\n"
    "v_mfma_f32_16x16x32_bf16 v[44:47], v[184:187], v[24:27], v[44:47]\n"
    "v_cvt_pk_bf16_f32 v38, v34, v38\n"
    "v_mfma_f32_16x16x32_bf16 v[44:47], v[188:191], v[28:31], v[44:47]\n"
    "v_cvt_pk_bf16_f32 v39, v35, v39\n"
    "v_mfma_f32_16x16x32_bf16 v[32:35], v[192:195], v[0:3], 0\n"
    "ds_write_addtid_b32 v36 offset:2048\n"
    "v_mfma_f32_16x16x32_bf16 v[32:35], v[196:199], v[4:7], v[32:35]\n"
    "ds_write_addtid_b32 v37 offset:2304\n"
    "v_mfma_f32_16x16x32_bf16 v[32:35], v[200:203], v[8:11], v[32:35]\n"
    "ds_write_addtid_b32 v38 offset:2560\n"
    "v_mfma_f32_16x16x32_bf16 v[32:35], v[204:207], v[12:15], v[32:35]\n"
    "ds_write_addtid_b32 v39 offset:2816\n"
    "v_mfma_f32_16x16x32_bf16 v[36:39], v[192:195], v[16:19], 0\n"
    "v_cvt_pk_bf16_f32 v44, v40, v44\n"
    "v_mfma_f32_16x16x32_bf16 v[36:39], v[196:199], v[20:23], v[36:39]\n"
    "v_cvt_pk_bf16_f32 v45, v41, v45\n"
    "v_mfma_f32_16x16x32_bf16 v[36:39], v[200:203], v[24:27], v[36:39]\n"
    "v_cvt_pk_bf16_f32 v46, v42, v46\n"
    "v_mfma_f32_16x16x32_bf16 v[36:39], v[204:207], v[28:31], v[36:39]\n"
    "v_cvt_pk_bf16_f32 v47, v43, v47\n"
    "v_mfma_f32_16x16x32_bf16 v[40:43], v[208:211], v[0:3], 0\n"
    "ds_write_addtid_b32 v44 offset:3072\n"
    "v_mfma_f32_16x16x32_bf16 v[40:43], v[212:215], v[4:7], v[40:43]\n"
    "ds_write_addtid_b32 v45 offset:3328\n"
    "v_mfma_f32_16x16x32_bf16 v[40:43], v[216:219], v[8:11], v[40:43]\n"
    "ds_write_addtid_b32 v46 offset:3584\n"
    "v_mfma_f32_16x16x32_bf16 v[40:43], v[220:223], v[12:15], v[40:43]\n"
    "ds_write_addtid_b32 v47 offset:3840\n"
    "v_mfma_f32_16x16x32_bf16 v[44:47], v[208:211], v[16:19], 0\n"
    "v_cvt_pk_bf16_f32 v36, v32, v36\n"
    "v_mfma_f32_16x16x32_bf16 v[44:47], v[212:215], v[20:23], v[44:47]\n"
    "v_cvt_pk_bf16_f32 v37, v33, v37\n"
    "v_mfma_f32_16x16x32_bf16 v[44:47], v[216:219], v[24:27], v[44:47]\n"
    "v_cvt_pk_bf16_f32 v38, v34, v38\n"
    "v_mfma_f32_16x16x32_bf16 v[44:47], v[220:223], v[28:31], v[44:47]\n"
    "v_cvt_pk_bf16_f32 v39, v35, v39\n"
    "v_mfma_f32_16x16x32_bf16 v[32:35], v[224:227], v[0:3], 0\n"
    "ds_write_addtid_b32 v36 offset:4096\n"
    "v_mfma_f32_16x16x32_bf16 v[32:35], v[228:231], v[4:7], v[32:35]\n"
    "ds_write_addtid_b32 v37 offset:4352\n"
    "v_mfma_f32_16x16x32_bf16 v[32:35], v[232:235], v[8:11], v[32:35]\n"
    "ds_write_addtid_b32 v38 offset:4608\n"
    "v_mfma_f32_16x16x32_bf16 v[32:35], v[236:239], v[12:15], v[32:35]\n"
    "ds_write_addtid_b32 v39 offset:4864\n"
    "v_mfma_f32_16x16x32_bf16 v[36:39], v[224:227], v[16:19], 0\n"
    "v_cvt_pk_bf16_f32 v44, v40, v44\n"
    "v_mfma_f32_16x16x32_bf16 v[36:39], v[228:231], v[20:23], v[36:39]\n"
    "v_cvt_pk_bf16_f32 v45, v41, v45\n"
    "v_mfma_f32_16x16x32_bf16 v[36:39], v[232:235], v[24:27], v[36:39]\n"
    "v_cvt_pk_bf16_f32 v46, v42, v46\n"
    "v_mfma_f32_16x16x32_bf16 v[36:39], v[236:239], v[28:31], v[36:39]\n"
    "v_cvt_pk_bf16_f32 v47, v43, v47\n"
    "v_mfma_f32_16x16x32_bf16 v[40:43], v[240:243], v[0:3], 0\n"
    "ds_write_addtid_b32 v44 offset:5120\n"
    "v_mfma_f32_16x16x32_bf16 v[40:43], v[244:247], v[4:7], v[40:43]\n"
    "ds_write_addtid_b32 v45 offset:5376\n"
    "v_mfma_f32_16x16x32_bf16 v[40:43], v[248:251], v[8:11], v[40:43]\n"
    "ds_write_addtid_b32 v46 offset:5632\n"
    "v_mfma_f32_16x16x32_bf16 v[40:43], v[252:255], v[12:15], v[40:43]\n"
    "ds_write_addtid_b32 v47 offset:5888\n"
    "v_mfma_f32_16x16x32_bf16 v[44:47], v[240:243], v[16:19], 0\n"
    "v_cvt_pk_bf16_f32 v36, v32, v36\n"
    "v_mfma_f32_16x16x32_bf16 v[44:47], v[244:247], v[20:23], v[44:47]\n"
    "v_cvt_pk_bf16_f32 v37, v33, v37\n"
    "v_mfma_f32_16x16x32_bf16 v[44:47], v[248:251], v[24:27], v[44:47]\n"
    "v_cvt_pk_bf16_f32 v38, v34, v38\n"
    "v_mfma_f32_16x16x32_bf16 v[44:47], v[252:255], v[28:31], v[44:47]\n"
    "v_cvt_pk_bf16_f32 v39, v35, v39\n"
    "buffer_load_dwordx4 v[32:35], v57, s[48:51], s56 offen offset:2048 sc1\n"
    "ds_write_addtid_b32 v36 offset:6144\n"
    "ds_write_addtid_b32 v37 offset:6400\n"
    "ds_write_addtid_b32 v38 offset:6656\n"
    "ds_write_addtid_b32 v39 offset:6912\n"
    "buffer_load_dwordx4 v[36:39], v57, s[48:51], s56 offen offset:3072 sc1\n"
    "s_nop 0\n"
    "v_cvt_pk_bf16_f32 v44, v40, v44\n"
    "v_cvt_pk_bf16_f32 v45, v41, v45\n"
    "v_cvt_pk_bf16_f32 v46, v42, v46\n"
    "v_cvt_pk_bf16_f32 v47, v43, v47\n"
    "ds_write_addtid_b32 v44 offset:7168\n"
    "ds_write_addtid_b32 v45 offset:7424\n"
    "ds_write_addtid_b32 v46 offset:7680\n"
    "ds_write_addtid_b32 v47 offset:7936\n"
    "s_setprio 0\n"
    "s_cbranch_vccz L_EG0_%=\n"
    "s_branch L_G1_%=\n"
    "L_IM0_%=:\n"
    "s_add_u32 m0, s35, 139392\n"
    "s_nop 0\n"
    "buffer_load_dwordx4 v57, s[48:51], s34 offen sc1 lds\n"
    "s_branch L_IM0r_%=\n"
    "L_PK_%=:\n"
    "s_cbranch_vccz L_D0r_%=\n"
    "buffer_load_dwordx4 v[48:51], v57, s[48:51], s56 offen sc1\n"
    "buffer_load_dwordx4 v[52:55], v57, s[48:51], s56 offen offset:1024 sc1\n"
    "s_branch L_D0r_%=\n"
    "L_CF_%=:\n"
    "v_mov_b64 v[40:41], s[12:13]\n"
    "v_mov_b64 v[42:43], s[12:13]\n"
    "s_mov_b64 exec, 255\n"
    "buffer_store_dwordx4 v[40:43], v57, s[48:51], s19 offen offset:3072 sc1\n"
    "s_mov_b64 exec, -1\n"
    "s_branch L_C1r_%=\n"
    "L_GLB_%=:\n"
    "s_waitcnt lgkmcnt(0)\n"
    "s_barrier\n"
    "s_setprio 0\n"
    "s_waitcnt vmcnt(3)\n"
    "v_and_b32 v0, s43, v48\n"
    "v_lshrrev_b32 v4, 16, v48\n"
    "ds_read_b128 v[0:3], v0 offset:65520\n"
    "ds_read_b128 v[4:7], v4 offset:65520\n"
    "v_and_b32 v8, s43, v49\n"
    "v_lshrrev_b32 v12, 16, v49\n"
    "ds_read_b128 v[8:11], v8 offset:65520\n"
    "ds_read_b128 v[12:15], v12 offset:65520\n"
    "s_add_u32 s34, s34, s35\n"
    "s_lshl_b32 s35, s35, 1\n"
    "s_add_u32 m0, s35, 139520\n"
    "s_nop 0\n"
    "buffer_load_dwordx4 v57, s[48:51], s34 offen sc1 lds\n"
    "buffer_load_dwordx4 v57, s[48:51], s34 offen offset:1024 sc1 lds\n"
    "v_and_b32 v16, s43, v50\n"
    "v_lshrrev_b32 v20, 16, v50\n"
    "ds_read_b128 v[16:19], v16 offset:65520\n"
    "ds_read_b128 v[20:23], v20 offset:65520\n"
    "v_and_b32 v24, s43, v51\n"
    "v_lshrrev_b32 v28, 16, v51\n"
    "ds_read_b128 v[24:27], v24 offset:65520\n"
    "ds_read_b128 v[28:31], v28 offset:65520\n"
    "s_waitcnt vmcnt(4)\n"
    "v_and_b32 v44, s43, v52\n"
    "v_lshrrev_b32 v40, 16, v52\n"
    "s_waitcnt lgkmcnt(6)\n"
    "s_setprio 2\n"
    "v_smfmac_f32_16x16x64_bf16 v[64:67], v[60:63], v[0:7], v59\n"
    "s_setprio 0\n"
    "ds_read_b128 v[0:3], v44 offset:65520\n"
    "ds_read_b128 v[4:7], v40 offset:65520\n"
    "s_add_u32 s56, s56, 32768\n"
    "buffer_load_dwordx4 v[48:51], v57, s[48:51], s56 offen sc1\n"
    "v_and_b32 v45, s43, v53\n"
    "v_lshrrev_b32 v41, 16, v53\n"
    "s_waitcnt lgkmcnt(6)\n"
    "s_setprio 2\n"
    "v_smfmac_f32_16x16x64_bf16 v[68:71], v[60:63], v[8:15], v59\n"
    "s_setprio 0\n"
    "ds_read_b128 v[8:11], v45 offset:65520\n"
    "ds_read_b128 v[12:15], v41 offset:65520\n"
    "v_and_b32 v46, s43, v54\n"
    "v_lshrrev_b32 v42, 16, v54\n"
    "s_waitcnt lgkmcnt(6)\n"
    "s_setprio 2\n"
    "v_smfmac_f32_16x16x64_bf16 v[72:75], v[60:63], v[16:23], v59\n"
    "s_setprio 0\n"
    "ds_read_b128 v[16:19], v46 offset:65520\n"
    "ds_read_b128 v[20:23], v42 offset:65520\n"
    "v_and_b32 v47, s43, v55\n"
    "v_lshrrev_b32 v43, 16, v55\n"
    "s_waitcnt lgkmcnt(6)\n"
    "s_setprio 2\n"
    "v_smfmac_f32_16x16x64_bf16 v[76:79], v[60:63], v[24:31], v59\n"
    "s_setprio 0\n"
    "v_cvt_pk_bf16_f32 v64, v64, v65\n"
    "v_cvt_pk_bf16_f32 v65, v68, v69\n"
    "v_cvt_pk_bf16_f32 v66, v66, v67\n"
    "v_cvt_pk_bf16_f32 v67, v70, v71\n"
    "v_min_u32 v64, -2, v64\n"
    "buffer_store_dwordx4 v[64:67], v57, s[48:51], s18 offen sc1\n"
    "ds_read_b128 v[24:27], v47 offset:65520\n"
    "ds_read_b128 v[28:31], v43 offset:65520\n"
    "s_waitcnt vmcnt(5)\n"
    "v_and_b32 v44, s43, v32\n"
    "v_lshrrev_b32 v40, 16, v32\n"
    "s_waitcnt lgkmcnt(6)\n"
    "s_setprio 2\n"
    "v_smfmac_f32_16x16x64_bf16 v[80:83], v[60:63], v[0:7], v59\n"
    "s_setprio 0\n"
    "ds_read_b128 v[0:3], v44 offset:65520\n"
    "ds_read_b128 v[4:7], v40 offset:65520\n"
    "v_and_b32 v45, s43, v33\n"
    "v_lshrrev_b32 v41, 16, v33\n"
    "s_waitcnt lgkmcnt(6)\n"
    "s_setprio 2\n"
    "v_smfmac_f32_16x16x64_bf16 v[84:87], v[60:63], v[8:15], v59\n"
    "s_setprio 0\n"
    "v_cvt_pk_bf16_f32 v72, v72, v73\n"
    "v_cvt_pk_bf16_f32 v73, v76, v77\n"
    "v_cvt_pk_bf16_f32 v74, v74, v75\n"
    "v_cvt_pk_bf16_f32 v75, v78, v79\n"
    "v_min_u32 v72, -2, v72\n"
    "ds_read_b128 v[8:11], v45 offset:65520\n"
    "ds_read_b128 v[12:15], v41 offset:65520\n"
    "v_and_b32 v46, s43, v34\n"
    "v_lshrrev_b32 v42, 16, v34\n"
    "s_waitcnt lgkmcnt(6)\n"
    "s_setprio 2\n"
    "v_smfmac_f32_16x16x64_bf16 v[88:91], v[60:63], v[16:23], v59\n"
    "s_setprio 0\n"
    "ds_read_b128 v[16:19], v46 offset:65520\n"
    "ds_read_b128 v[20:23], v42 offset:65520\n"
    "v_and_b32 v47, s43, v35\n"
    "v_lshrrev_b32 v43, 16, v35\n"
    "s_waitcnt lgkmcnt(6)\n"
    "s_setprio 2\n"
    "v_smfmac_f32_16x16x64_bf16 v[92:95], v[60:63], v[24:31], v59\n"
    "s_setprio 0\n"
    "v_cvt_pk_bf16_f32 v80, v80, v81\n"
    "v_cvt_pk_bf16_f32 v81, v84, v85\n"
    "v_cvt_pk_bf16_f32 v82, v82, v83\n"
    "v_cvt_pk_bf16_f32 v83, v86, v87\n"
    "v_min_u32 v80, -2, v80\n"
    "ds_read_b128 v[24:27], v47 offset:65520\n"
    "ds_read_b128 v[28:31], v43 offset:65520\n"
    "s_waitcnt lgkmcnt(6)\n"
    "s_setprio 2\n"
    "v_smfmac_f32_16x16x64_bf16 v[96:99], v[60:63], v[0:7], v59\n"
    "s_setprio 0\n"
    "s_waitcnt lgkmcnt(4)\n"
    "s_setprio 2\n"
    "v_smfmac_f32_16x16x64_bf16 v[100:103], v[60:63], v[8:15], v59\n"
    "s_setprio 0\n"
    "v_cvt_pk_bf16_f32 v76, v88, v89\n"
    "v_cvt_pk_bf16_f32 v77, v92, v93\n"
    "v_cvt_pk_bf16_f32 v78, v90, v91\n"
    "v_cvt_pk_bf16_f32 v79, v94, v95\n"
    "v_min_u32 v76, -2, v76\n"
    "s_waitcnt vmcnt(1)\n"
    "v_mov_b32 v52, v36\n"
    "v_mov_b32 v53, v37\n"
    "v_mov_b32 v54, v38\n"
    "v_mov_b32 v55, v39\n"
    "s_waitcnt lgkmcnt(2)\n"
    "s_setprio 2\n"
    "v_smfmac_f32_16x16x64_bf16 v[104:107], v[60:63], v[16:23], v59\n"
    "s_setprio 0\n"
    "v_lshrrev_b32 v88, 8, v57\n"
    "s_lshl_b32 s65, s11, 22\n"
    "v_min_u32 v88, 2, v88\n"
    "s_sub_u32 s17, s18, s65\n"
    "v_add3_u32 v88, v88, s11, 1\n"
    "s_add_u32 s17, s17, 7168\n"
    "v_and_b32 v88, 3, v88\n"
    "v_lshlrev_b32 v88, 22, v88\n"
    "buffer_load_dwordx2 v[90:91], v88, s[48:51], s17 offen sc1\n"
    "s_waitcnt lgkmcnt(0)\n"
    "s_setprio 2\n"
    "v_smfmac_f32_16x16x64_bf16 v[108:111], v[60:63], v[24:31], v59\n"
    "s_setprio 0\n"
    "v_cvt_pk_bf16_f32 v68, v96, v97\n"
    "v_cvt_pk_bf16_f32 v69, v100, v101\n"
    "v_cvt_pk_bf16_f32 v70, v98, v99\n"
    "v_cvt_pk_bf16_f32 v71, v102, v103\n"
    "v_min_u32 v68, -2, v68\n"
    "v_cvt_pk_bf16_f32 v92, v104, v105\n"
    "s_nop 0\n"
    "v_cvt_pk_bf16_f32 v93, v108, v109\n"
    "v_cvt_pk_bf16_f32 v94, v106, v107\n"
    "v_cvt_pk_bf16_f32 v95, v110, v111\n"
    "v_min_u32 v92, -2, v92\n"
    "s_setprio 1\n"
    "ds_read_b64_tr_b16 v[0:1], v56\n"
    "ds_read_b64_tr_b16 v[2:3], v56 offset:64\n"
    "ds_read_b64_tr_b16 v[4:5], v56 offset:512\n"
    "ds_read_b64_tr_b16 v[6:7], v56 offset:576\n"
    "ds_read_b64_tr_b16 v[8:9], v56 offset:1024\n"
    "ds_read_b64_tr_b16 v[10:11], v56 offset:1088\n"
    "ds_read_b64_tr_b16 v[12:13], v56 offset:1536\n"
    "ds_read_b64_tr_b16 v[14:15], v56 offset:1600\n"
    "ds_read_b64_tr_b16 v[16:17], v56 offset:8\n"
    "ds_read_b64_tr_b16 v[18:19], v56 offset:72\n"
    "ds_read_b64_tr_b16 v[20:21], v56 offset:520\n"
    "ds_read_b64_tr_b16 v[22:23], v56 offset:584\n"
    "ds_read_b64_tr_b16 v[24:25], v56 offset:1032\n"
    "ds_read_b64_tr_b16 v[26:27], v56 offset:1096\n"
    "ds_read_b64_tr_b16 v[28:29], v56 offset:1544\n"
    "ds_read_b64_tr_b16 v[30:31], v56 offset:1608\n"
    "s_mov_b32 m0, s54\n"
    "s_waitcnt lgkmcnt(14)\n"
    "v_mfma_f32_16x16x32_bf16 v[32:35], v[128:131], v[0:3], 0\n"
    "s_waitcnt lgkmcnt(12)\n"
    "v_mfma_f32_16x16x32_bf16 v[32:35], v[132:135], v[4:7], v[32:35]\n"
    "buffer_store_dwordx4 v[72:75], v57, s[48:51], s18 offen offset:1024 sc1\n"
    "s_waitcnt lgkmcnt(10)\n"
    "v_mfma_f32_16x16x32_bf16 v[32:35], v[136:139], v[8:11], v[32:35]\n"
    "s_waitcnt lgkmcnt(8)\n"
    "v_mfma_f32_16x16x32_bf16 v[32:35], v[140:143], v[12:15], v[32:35]\n"
    "s_waitcnt lgkmcnt(6)\n"
    "v_mfma_f32_16x16x32_bf16 v[36:39], v[128:131], v[16:19], 0\n"
    "s_lshl_b32 s65, s11, 22\n"
    "s_sub_u32 s57, s18, s65\n"
    "s_waitcnt lgkmcnt(4)\n"
    "v_mfma_f32_16x16x32_bf16 v[36:39], v[132:135], v[20:23], v[36:39]\n"
    "s_waitcnt lgkmcnt(2)\n"
    "v_mfma_f32_16x16x32_bf16 v[36:39], v[136:139], v[24:27], v[36:39]\n"
    "s_sub_u32 s65, s11, 1\n"
    "s_and_b32 s65, s65, 3\n"
    "s_lshl_b32 s65, s65, 22\n"
    "s_add_u32 s59, s65, s57\n"
    "s_waitcnt lgkmcnt(0)\n"
    "v_mfma_f32_16x16x32_bf16 v[36:39], v[140:143], v[28:31], v[36:39]\n"
    "v_mfma_f32_16x16x32_bf16 v[40:43], v[144:147], v[0:3], 0\n"
    "s_sub_u32 s65, s11, 2\n"
    "s_and_b32 s65, s65, 3\n"
    "s_lshl_b32 s65, s65, 22\n"
    "s_add_u32 s60, s65, s57\n"
    "s_add_u32 s60, s60, 2048\n"
    "v_mfma_f32_16x16x32_bf16 v[40:43], v[148:151], v[4:7], v[40:43]\n"
    "buffer_store_dwordx4 v[80:83], v57, s[48:51], s18 offen offset:2048 sc1\n"
    "v_mfma_f32_16x16x32_bf16 v[40:43], v[152:155], v[8:11], v[40:43]\n"
    "s_sub_u32 s65, s11, 3\n"
    "s_and_b32 s65, s65, 3\n"
    "s_lshl_b32 s65, s65, 22\n"
    "s_add_u32 s61, s65, s57\n"
    "s_add_u32 s61, s61, 4096\n"
    "v_mfma_f32_16x16x32_bf16 v[40:43], v[156:159], v[12:15], v[40:43]\n"
    "v_mfma_f32_16x16x32_bf16 v[44:47], v[144:147], v[16:19], 0\n"
    "v_cvt_pk_bf16_f32 v36, v32, v36\n"
    "s_and_b32 s63, %[wg], 0xc0\n"
    "s_lshl_b32 s63, s63, 16\n"
    "s_lshl_b32 s65, s10, 4\n"
    "s_add_u32 s63, s63, s65\n"
    "v_mfma_f32_16x16x32_bf16 v[44:47], v[148:151], v[20:23], v[44:47]\n"
    "v_cvt_pk_bf16_f32 v37, v33, v37\n"
    "v_and_b32 v128, s43, v52\n"
    "v_lshrrev_b32 v132, 16, v52\n"
    "ds_read_b128 v[128:131], v128 offset:65520\n"
    "ds_read_b128 v[132:135], v132 offset:65520\n"
    "v_mfma_f32_16x16x32_bf16 v[44:47], v[152:155], v[24:27], v[44:47]\n"
    "v_cvt_pk_bf16_f32 v38, v34, v38\n"
    "s_and_b32 s65, %[wg], 0x38\n"
    "s_lshl_b32 s65, s65, 10\n"
    "s_add_u32 s63, s63, s65\n"
    "s_add_u32 s62, s19, 2048\n"
    "v_mfma_f32_16x16x32_bf16 v[44:47], v[156:159], v[28:31], v[44:47]\n"
    "v_cvt_pk_bf16_f32 v39, v35, v39\n"
    "v_mfma_f32_16x16x32_bf16 v[32:35], v[160:163], v[0:3], 0\n"
    "ds_write_addtid_b32 v36 offset:0\n"
    "s_and_b32 s65, %[wg], 7\n"
    "s_lshl_b32 s65, s65, 7\n"
    "s_add_u32 s63, s63, s65\n"
    "s_add_u32 s63, s63, 2103296\n"
    "v_mfma_f32_16x16x32_bf16 v[32:35], v[164:167], v[4:7], v[32:35]\n"
    "ds_write_addtid_b32 v37 offset:256\n"
    "buffer_store_dwordx4 v[76:79], v57, s[48:51], s18 offen offset:3072 sc1\n"
    "v_mfma_f32_16x16x32_bf16 v[32:35], v[168:171], v[8:11], v[32:35]\n"
    "ds_write_addtid_b32 v38 offset:512\n"
    "s_lshr_b32 s53, %[wg], 1\n"
    "s_lshl_b32 s53, s53, 2\n"
    "s_and_b32 s65, %[wg], 1\n"
    "s_add_u32 s53, s53, s65\n"
    "s_lshl_b32 s53, s53, 3\n"
    "s_add_u32 s53, s53, s10\n"
    "s_lshl_b32 s53, s53, 1\n"
    "s_lshl_b32 s98, s98, 16\n"
    "v_mfma_f32_16x16x32_bf16 v[32:35], v[172:175], v[12:15], v[32:35]\n"
    "ds_write_addtid_b32 v39 offset:768\n"
    "v_mfma_f32_16x16x32_bf16 v[36:39], v[160:163], v[16:19], 0\n"
    "v_cvt_pk_bf16_f32 v44, v40, v44\n"
    "v_mfma_f32_16x16x32_bf16 v[36:39], v[164:167], v[20:23], v[36:39]\n"
    "v_cvt_pk_bf16_f32 v45, v41, v45\n"
    "v_mfma_f32_16x16x32_bf16 v[36:39], v[168:171], v[24:27], v[36:39]\n"
    "v_cvt_pk_bf16_f32 v46, v42, v46\n"
    "v_mfma_f32_16x16x32_bf16 v[36:39], v[172:175], v[28:31], v[36:39]\n"
    "v_cvt_pk_bf16_f32 v47, v43, v47\n"
    "v_mfma_f32_16x16x32_bf16 v[40:43], v[176:179], v[0:3], 0\n"
    "ds_write_addtid_b32 v44 offset:1024\n"
    "v_mfma_f32_16x16x32_bf16 v[40:43], v[180:183], v[4:7], v[40:43]\n"
    "ds_write_addtid_b32 v45 offset:1280\n"
    "buffer_store_dwordx4 v[68:71], v57, s[48:51], s19 offen sc1\n"
    "v_mfma_f32_16x16x32_bf16 v[40:43], v[184:187], v[8:11], v[40:43]\n"
    "ds_write_addtid_b32 v46 offset:1536\n"
    "v_mfma_f32_16x16x32_bf16 v[40:43], v[188:191], v[12:15], v[40:43]\n"
    "ds_write_addtid_b32 v47 offset:1792\n"
    "v_mfma_f32_16x16x32_bf16 v[44:47], v[176:179], v[16:19], 0\n"
    "v_cvt_pk_bf16_f32 v36, v32, v36\n"
    "v_and_b32 v160, s43, v53\n"
    "v_lshrrev_b32 v164, 16, v53\n"
    "ds_read_b128 v[160:163], v160 offset:65520\n"
    "ds_read_b128 v[164:167], v164 offset:65520\n"
    "v_mfma_f32_16x16x32_bf16 v[44:47], v[180:183], v[20:23], v[44:47]\n"
    "v_cvt_pk_bf16_f32 v37, v33, v37\n"
    "v_mfma_f32_16x16x32_bf16 v[44:47], v[184:187], v[24:27], v[44:47]\n"
    "v_cvt_pk_bf16_f32 v38, v34, v38\n"
    "v_mfma_f32_16x16x32_bf16 v[44:47], v[188:191], v[28:31], v[44:47]\n"
    "v_cvt_pk_bf16_f32 v39, v35, v39\n"
    "v_mfma_f32_16x16x32_bf16 v[32:35], v[192:195], v[0:3], 0\n"
    "ds_write_addtid_b32 v36 offset:2048\n"
    "v_mfma_f32_16x16x32_bf16 v[32:35], v[196:199], v[4:7], v[32:35]\n"
    "ds_write_addtid_b32 v37 offset:2304\n"
    "buffer_store_dwordx4 v[92:95], v57, s[48:51], s19 offen offset:1024 sc1\n"
    "v_mfma_f32_16x16x32_bf16 v[32:35], v[200:203], v[8:11], v[32:35]\n"
    "ds_write_addtid_b32 v38 offset:2560\n"
    "v_mfma_f32_16x16x32_bf16 v[32:35], v[204:207], v[12:15], v[32:35]\n"
    "ds_write_addtid_b32 v39 offset:2816\n"
    "v_mfma_f32_16x16x32_bf16 v[36:39], v[192:195], v[16:19], 0\n"
    "v_cvt_pk_bf16_f32 v44, v40, v44\n"
    "v_mfma_f32_16x16x32_bf16 v[36:39], v[196:199], v[20:23], v[36:39]\n"
    "v_cvt_pk_bf16_f32 v45, v41, v45\n"
    "v_mfma_f32_16x16x32_bf16 v[36:39], v[200:203], v[24:27], v[36:39]\n"
    "v_cvt_pk_bf16_f32 v46, v42, v46\n"
    "v_mfma_f32_16x16x32_bf16 v[36:39], v[204:207], v[28:31], v[36:39]\n"
    "v_cvt_pk_bf16_f32 v47, v43, v47\n"
    "v_mfma_f32_16x16x32_bf16 v[40:43], v[208:211], v[0:3], 0\n"
    "ds_write_addtid_b32 v44 offset:3072\n"
    "s_mul_i32 s66, s10, 800\n"
    "s_add_u32 s66, s66, 156032\n"
    "v_mfma_f32_16x16x32_bf16 v[40:43], v[212:215], v[4:7], v[40:43]\n"
    "ds_write_addtid_b32 v45 offset:3328\n"
    "v_bfe_u32 v148, v57, 4, 4\n"
    "v_mfma_f32_16x16x32_bf16 v[40:43], v[216:219], v[8:11], v[40:43]\n"
    "ds_write_addtid_b32 v46 offset:3584\n"
    "v_and_b32 v149, 12, v148\n"
    "v_mfma_f32_16x16x32_bf16 v[40:43], v[220:223], v[12:15], v[40:43]\n"
    "ds_write_addtid_b32 v47 offset:3840\n"
    "v_lshl_add_u32 v148, v148, 2, s66\n"
    "v_mfma_f32_16x16x32_bf16 v[44:47], v[208:211], v[16:19], 0\n"
    "v_cvt_pk_bf16_f32 v36, v32, v36\n"
    "v_add_u32 v149, s66, v149\n"
    "v_mfma_f32_16x16x32_bf16 v[44:47], v[212:215], v[20:23], v[44:47]\n"
    "v_cvt_pk_bf16_f32 v37, v33, v37\n"
    "ds_read2_b32 v[176:177], v149 offset0:96 offset1:196\n"
    "v_mfma_f32_16x16x32_bf16 v[44:47], v[216:219], v[24:27], v[44:47]\n"
    "v_cvt_pk_bf16_f32 v38, v34, v38\n"
    "ds_read2_b32 v[178:179], v148 offset0:0 offset1:100\n"
    "v_mfma_f32_16x16x32_bf16 v[44:47], v[220:223], v[28:31], v[44:47]\n"
    "v_cvt_pk_bf16_f32 v39, v35, v39\n"
    "ds_read2_b32 v[180:181], v148 offset0:16 offset1:116\n"
    "v_mfma_f32_16x16x32_bf16 v[32:35], v[224:227], v[0:3], 0\n"
    "ds_write_addtid_b32 v36 offset:4096\n"
    "ds_read2_b32 v[182:183], v148 offset0:32 offset1:132\n"
    "v_mfma_f32_16x16x32_bf16 v[32:35], v[228:231], v[4:7], v[32:35]\n"
    "ds_write_addtid_b32 v37 offset:4352\n"
    "ds_read2_b32 v[192:193], v148 offset0:48 offset1:148\n"
    "v_mfma_f32_16x16x32_bf16 v[32:35], v[232:235], v[8:11], v[32:35]\n"
    "ds_write_addtid_b32 v38 offset:4608\n"
    "ds_read2_b32 v[194:195], v148 offset0:64 offset1:164\n"
    "v_mfma_f32_16x16x32_bf16 v[32:35], v[236:239], v[12:15], v[32:35]\n"
    "ds_write_addtid_b32 v39 offset:4864\n"
    "ds_read2_b32 v[196:197], v148 offset0:80 offset1:180\n"
    "v_mfma_f32_16x16x32_bf16 v[36:39], v[224:227], v[16:19], 0\n"
    "v_cvt_pk_bf16_f32 v44, v40, v44\n"
    "v_lshrrev_b32 v150, 4, v57\n"
    "v_mfma_f32_16x16x32_bf16 v[36:39], v[228:231], v[20:23], v[36:39]\n"
    "v_cvt_pk_bf16_f32 v45, v41, v45\n"
    "v_and_b32 v144, 48, v57\n"
    "v_mfma_f32_16x16x32_bf16 v[36:39], v[232:235], v[24:27], v[36:39]\n"
    "v_cvt_pk_bf16_f32 v46, v42, v46\n"
    "v_bitop3_b32 v145, v57, v150, 48 bitop3:0x28\n"
    "v_mfma_f32_16x16x32_bf16 v[36:39], v[236:239], v[28:31], v[36:39]\n"
    "v_cvt_pk_bf16_f32 v47, v43, v47\n"
    "v_lshl_or_b32 v145, v144, 4, v145\n"
    "v_mfma_f32_16x16x32_bf16 v[40:43], v[240:243], v[0:3], 0\n"
    "ds_write_addtid_b32 v44 offset:5120\n"
    "v_and_b32 v144, 0xc0, v57\n"
    "v_mfma_f32_16x16x32_bf16 v[40:43], v[244:247], v[4:7], v[40:43]\n"
    "ds_write_addtid_b32 v45 offset:5376\n"
    "v_add3_u32 v145, v145, v144, s54\n"
    "v_mfma_f32_16x16x32_bf16 v[40:43], v[248:251], v[8:11], v[40:43]\n"
    "ds_write_addtid_b32 v46 offset:5632\n"
    "v_add_u32 v151, 65024, v145\n"
    "v_mfma_f32_16x16x32_bf16 v[40:43], v[252:255], v[12:15], v[40:43]\n"
    "ds_write_addtid_b32 v47 offset:5888\n"
    "v_and_b32 v208, s43, v54\n"
    "v_lshrrev_b32 v212, 16, v54\n"
    "ds_read_b128 v[208:211], v208 offset:65520\n"
    "ds_read_b128 v[212:215], v212 offset:65520\n"
    "v_mfma_f32_16x16x32_bf16 v[44:47], v[240:243], v[16:19], 0\n"
    "v_cvt_pk_bf16_f32 v36, v32, v36\n"
    "v_and_b32 v224, s43, v55\n"
    "v_lshrrev_b32 v228, 16, v55\n"
    "ds_read_b128 v[224:227], v224 offset:65520\n"
    "ds_read_b128 v[228:231], v228 offset:65520\n"
    "v_mfma_f32_16x16x32_bf16 v[44:47], v[244:247], v[20:23], v[44:47]\n"
    "v_cvt_pk_bf16_f32 v37, v33, v37\n"
    "v_mfma_f32_16x16x32_bf16 v[44:47], v[248:251], v[24:27], v[44:47]\n"
    "v_cvt_pk_bf16_f32 v38, v34, v38\n"
    "v_mfma_f32_16x16x32_bf16 v[44:47], v[252:255], v[28:31], v[44:47]\n"
    "v_cvt_pk_bf16_f32 v39, v35, v39\n"
    "ds_write_addtid_b32 v36 offset:6144\n"
    "ds_write_addtid_b32 v37 offset:6400\n"
    "ds_write_addtid_b32 v38 offset:6656\n"
    "ds_write_addtid_b32 v39 offset:6912\n"
    "s_nop 2\n"
    "v_cvt_pk_bf16_f32 v44, v40, v44\n"
    "v_cvt_pk_bf16_f32 v45, v41, v45\n"
    "v_cvt_pk_bf16_f32 v46, v42, v46\n"
    "v_cvt_pk_bf16_f32 v47, v43, v47\n"
    "ds_write_addtid_b32 v44 offset:7168\n"
    "ds_write_addtid_b32 v45 offset:7424\n"
    "ds_write_addtid_b32 v46 offset:7680\n"
    "ds_write_addtid_b32 v47 offset:7936\n"
    "s_waitcnt lgkmcnt(15)\n"
    "v_smfmac_f32_16x16x64_bf16 v[112:115], v[60:63], v[128:135], v59\n"
    "s_waitcnt lgkmcnt(15)\n"
    "v_smfmac_f32_16x16x64_bf16 v[116:119], v[60:63], v[160:167], v59\n"
    "s_waitcnt lgkmcnt(10)\n"
    "v_smfmac_f32_16x16x64_bf16 v[120:123], v[60:63], v[208:215], v59\n"
    "s_waitcnt lgkmcnt(8)\n"
    "v_smfmac_f32_16x16x64_bf16 v[124:127], v[60:63], v[224:231], v59\n"
    "v_and_b32 v16, s43, v48\n"
    "v_lshrrev_b32 v20, 16, v48\n"
    "v_and_b32 v24, s43, v49\n"
    "v_lshrrev_b32 v28, 16, v49\n"
    "v_and_b32 v32, s43, v50\n"
    "v_lshrrev_b32 v36, 16, v50\n"
    "v_and_b32 v40, s43, v51\n"
    "v_lshrrev_b32 v44, 16, v51\n"
    "s_waitcnt lgkmcnt(0)\n"
    "s_barrier\n"
    "s_setprio 1\n"
    "ds_read_b128 v[16:19], v16\n"
    "ds_read_b128 v[20:23], v20\n"
    "ds_read_b128 v[24:27], v24\n"
    "ds_read_b128 v[28:31], v28\n"
    "ds_read_b128 v[32:35], v32\n"
    "ds_read_b128 v[36:39], v36\n"
    "ds_read_b128 v[40:43], v40\n"
    "ds_read_b128 v[44:47], v44\n"
    "s_waitcnt vmcnt(5)\n"
    "s_mov_b32 s66, 65536\n"
    "L_p2Ba_%=:\n"
    "v_cmp_ne_u64_e64 s[14:15], v[90:91], s[12:13]\n"
    "s_nop 3\n"
    "s_cmp_eq_u64 s[14:15], 0\n"
    "s_cbranch_scc1 L_p2Bb_%=\n"
    "s_sleep 2\n"
    "buffer_load_dwordx2 v[90:91], v88, s[48:51], s17 offen sc1\n"
    "s_waitcnt vmcnt(0)\n"
    "s_sub_u32 s66, s66, 1\n"
    "s_cmp_lg_u32 s66, 0\n"
    "s_cbranch_scc1 L_p2Ba_%=\n"
    "L_p2Bb_%=:\n"
    "ds_write_b128 v151, v[152:155] offset:1024\n"
    "s_mov_b32 exec_lo, 0xeeeeeeee\n"
    "s_mov_b32 exec_hi, 0xeeeeeeee\n"
    "v_mov_b32 v176, 1000\n"
    "v_mov_b32 v177, 1000\n"
    "s_mov_b64 exec, -1\n"
    "v_lshlrev_b32 v0, 2, v176\n"
    "v_lshlrev_b32 v1, 2, v177\n"
    "v_bitop3_b32 v0, v0, v150, 48 bitop3:0x28\n"
    "v_bitop3_b32 v1, v1, v150, 48 bitop3:0x28\n"
    "v_lshl_or_b32 v0, v176, 6, v0\n"
    "v_lshl_or_b32 v1, v177, 6, v1\n"
    "ds_write_b128 v151, v[168:171] offset:2048\n"
    "v_lshlrev_b32 v2, 2, v178\n"
    "v_lshlrev_b32 v3, 2, v179\n"
    "v_bitop3_b32 v2, v2, v150, 48 bitop3:0x28\n"
    "v_bitop3_b32 v3, v3, v150, 48 bitop3:0x28\n"
    "v_lshl_or_b32 v2, v178, 6, v2\n"
    "v_lshl_or_b32 v3, v179, 6, v3\n"
    "v_lshlrev_b32 v4, 2, v180\n"
    "ds_write_b128 v151, v[184:187] offset:3072\n"
    "v_lshlrev_b32 v5, 2, v181\n"
    "v_bitop3_b32 v4, v4, v150, 48 bitop3:0x28\n"
    "v_bitop3_b32 v5, v5, v150, 48 bitop3:0x28\n"
    "v_lshl_or_b32 v4, v180, 6, v4\n"
    "v_lshl_or_b32 v5, v181, 6, v5\n"
    "v_lshlrev_b32 v6, 2, v182\n"
    "v_lshlrev_b32 v7, 2, v183\n"
    "ds_write_b128 v151, v[200:203] offset:4096\n"
    "v_bitop3_b32 v6, v6, v150, 48 bitop3:0x28\n"
    "v_bitop3_b32 v7, v7, v150, 48 bitop3:0x28\n"
    "v_lshl_or_b32 v6, v182, 6, v6\n"
    "v_lshl_or_b32 v7, v183, 6, v7\n"
    "v_lshlrev_b32 v8, 2, v192\n"
    "v_lshlrev_b32 v9, 2, v193\n"
    "v_bitop3_b32 v8, v8, v150, 48 bitop3:0x28\n"
    "s_waitcnt lgkmcnt(4)\n"
    "s_barrier\n"
    "ds_write_b128 v151, v[216:219] offset:5120\n"
    "v_smfmac_f32_16x16x64_bf16 v[112:115], v[60:63], v[16:23], v59\n"
    "v_smfmac_f32_16x16x64_bf16 v[116:119], v[60:63], v[24:31], v59\n"
    "v_smfmac_f32_16x16x64_bf16 v[120:123], v[60:63], v[32:39], v59\n"
    "v_smfmac_f32_16x16x64_bf16 v[124:127], v[60:63], v[40:47], v59\n"
    "v_bitop3_b32 v9, v9, v150, 48 bitop3:0x28\n"
    "v_lshl_or_b32 v8, v192, 6, v8\n"
    "v_lshl_or_b32 v9, v193, 6, v9\n"
    "ds_write_b128 v151, v[232:235] offset:6144\n"
    "v_lshlrev_b32 v10, 2, v194\n"
    "v_lshlrev_b32 v11, 2, v195\n"
    "v_bitop3_b32 v10, v10, v150, 48 bitop3:0x28\n"
    "v_bitop3_b32 v11, v11, v150, 48 bitop3:0x28\n"
    "v_lshl_or_b32 v10, v194, 6, v10\n"
    "v_lshl_or_b32 v11, v195, 6, v11\n"
    "v_lshlrev_b32 v12, 2, v196\n"
    "ds_write_b128 v151, v[248:251] offset:7168\n"
    "v_lshlrev_b32 v13, 2, v197\n"
    "v_bitop3_b32 v12, v12, v150, 48 bitop3:0x28\n"
    "v_bitop3_b32 v13, v13, v150, 48 bitop3:0x28\n"
    "v_lshl_or_b32 v12, v196, 6, v12\n"
    "v_lshl_or_b32 v13, v197, 6, v13\n"
    "v_bfe_u32 v58, v57, 2, 4\n"
    "v_and_or_b32 v58, v150, 48, v58\n"
    "ds_write_b128 v151, v[136:139]\n"
    "v_add_u32 v58, v144, v58\n"
    "v_add_u32 v56, 131072, v57\n"
    "v_add_u32 v58, 139392, v58\n"
    "v_lshrrev_b32 v105, 1, v57\n"
    "v_xor_b32 v105, v105, v57\n"
    "v_bfe_u32 v105, v105, 5, 3\n"
    "v_bfe_u32 v106, v57, 5, 2\n"
    "ds_write_b128 v145, v[140:143]\n"
    "buffer_load_dwordx4 v[64:67], v57, s[48:51], s59 offen sc1\n"
    "v_and_or_b32 v105, v105, 4, v106\n"
    "v_mov_b32 v106, 0x3f80\n"
    "v_cmp_eq_u32_e64 vcc, 0, v105\n"
    "v_cmp_eq_u32_e64 s[14:15], 1, v105\n"
    "v_mov_b32 v104, 0xbdd2d3e8\n"
    "v_lshrrev_b32 v110, 4, v57\n"
    "v_cndmask_b32_e64 v96, 0, v106, vcc\n"
    "ds_write_b128 v145, v[156:159] offset:1024\n"
    "buffer_load_dwordx4 v[72:75], v57, s[48:51], s59 offen offset:1024 sc1\n"
    "v_cndmask_b32_e64 v97, 0, v106, s[14:15]\n"
    "v_cmp_eq_u32_e64 vcc, 2, v105\n"
    "v_cmp_eq_u32_e64 s[14:15], 3, v105\n"
    "v_lshlrev_b32 v108, 12, v57\n"
    "v_bfe_u32 v105, v57, 4, 1\n"
    "v_cndmask_b32_e64 v98, 0, v106, vcc\n"
    "v_cndmask_b32_e64 v99, 0, v106, s[14:15]\n"
    "ds_write_b128 v145, v[172:175] offset:2048\n"
    "buffer_load_dwordx4 v[68:71], v57, s[48:51], s60 offen sc1\n"
    "v_mul_u32_u24 v105, 0x5555, v105\n"
    "v_or_b32 v106, 0xaaaa, v105\n"
    "v_and_b32 v110, 32, v110\n"
    "v_lshl_add_u32 v111, s10, 2, v110\n"
    "v_add_u32 v111, 163264, v111\n"
    "v_add_u32 v110, s53, v110\n"
    "ds_read_b32 v111, v111\n"
    "ds_write_b128 v145, v[188:191] offset:3072\n"
    "buffer_load_dwordx4 v[76:79], v57, s[48:51], s60 offen offset:1024 sc1\n"
    "v_lshlrev_b32_e64 v100, 16, s24\n"
    "s_and_b32 s24, s24, 0xffff0000\n"
    "v_lshlrev_b32_e64 v102, 16, s25\n"
    "s_and_b32 s25, s25, 0xffff0000\n"
    "v_mov_b32 v84, s20\n"
    "v_mov_b32 v85, s20\n"
    "v_mov_b32 v86, s21\n"
    "v_mov_b32 v87, s21\n"
    "v_mov_b32 v101, s24\n"
    "v_mov_b32 v103, s25\n"
    "s_mov_b32 exec_lo, 0xffff0000\n"
    "s_mov_b32 exec_hi, 0xffff0000\n"
    "v_lshlrev_b32_e64 v100, 16, s100\n"
    "s_and_b32 s100, s100, 0xffff0000\n"
    "v_lshlrev_b32_e64 v102, 16, s101\n"
    "s_and_b32 s101, s101, 0xffff0000\n"
    "v_mov_b32 v84, s22\n"
    "v_mov_b32 v85, s22\n"
    "v_mov_b32 v86, s23\n"
    "v_mov_b32 v87, s23\n"
    "v_mov_b32 v101, s100\n"
    "v_mov_b32 v103, s101\n"
    "s_mov_b64 exec, -1\n"
    "ds_write_b128 v145, v[204:207] offset:4096\n"
    "buffer_load_dwordx4 v[80:83], v57, s[48:51], s61 offen sc1\n"
    "ds_write_b128 v145, v[220:223] offset:5120\n"
    "buffer_load_dwordx4 v[88:91], v57, s[48:51], s61 offen offset:1024 sc1\n"
    "ds_write_b128 v145, v[236:239] offset:6144\n"
    "s_cmp_eq_u32 s10, 7\n"
    "s_cbranch_scc1 L_nq_%=\n"
    "ds_write_b128 v145, v[252:255] offset:7168\n"
    "L_nq_%=:\n"
    "s_waitcnt lgkmcnt(0)\n"
    "s_barrier\n"
    "s_setprio 2\n"
    "s_setprio 3\n"
    "s_waitcnt vmcnt(0)\n"
    "s_mov_b32 s66, 65536\n"
    "L_iaB_%=:\n"
    "v_max3_u32 v14, v64, v72, v68\n"
    "v_max3_u32 v15, v76, v80, v88\n"
    "v_max_u32 v14, v14, v15\n"
    "v_cmp_eq_u32_e64 vcc, -1, v14\n"
    "s_nop 3\n"
    "s_cbranch_vccz L_ibB_%=\n"
    "s_sleep 4\n"
    "buffer_load_dwordx4 v[64:67], v57, s[48:51], s59 offen sc1\n"
    "buffer_load_dwordx4 v[72:75], v57, s[48:51], s59 offen offset:1024 sc1\n"
    "buffer_load_dwordx4 v[68:71], v57, s[48:51], s60 offen sc1\n"
    "buffer_load_dwordx4 v[76:79], v57, s[48:51], s60 offen offset:1024 sc1\n"
    "buffer_load_dwordx4 v[80:83], v57, s[48:51], s61 offen sc1\n"
    "buffer_load_dwordx4 v[88:91], v57, s[48:51], s61 offen offset:1024 sc1\n"
    "s_waitcnt vmcnt(0)\n"
    "s_sub_u32 s66, s66, 1\n"
    "s_cmp_lg_u32 s66, 0\n"
    "s_cbranch_scc1 L_iaB_%=\n"
    "L_ibB_%=:\n"
    "v_smfmac_f32_16x16x64_bf16 v[112:115], v[96:99], v[64:71], v105\n"
    "v_smfmac_f32_16x16x64_bf16 v[116:119], v[96:99], v[64:71], v106\n"
    "v_smfmac_f32_16x16x64_bf16 v[120:123], v[96:99], v[72:79], v105\n"
    "v_smfmac_f32_16x16x64_bf16 v[124:127], v[96:99], v[72:79], v106\n"
    "v_smfmac_f32_16x16x64_bf16 v[112:115], v[96:99], v[80:87], v105\n"
    "v_smfmac_f32_16x16x64_bf16 v[116:119], v[96:99], v[80:87], v106\n"
    "v_smfmac_f32_16x16x64_bf16 v[120:123], v[96:99], v[84:91], v105\n"
    "v_smfmac_f32_16x16x64_bf16 v[124:127], v[96:99], v[84:91], v106\n"
    "s_nop 4\n"
    "v_mul_f32 v64, v112, v112\n"
    "v_mul_f32 v65, v113, v113\n"
    "v_mul_f32 v66, v114, v114\n"
    "v_mul_f32 v67, v115, v115\n"
    "v_mul_f32 v68, v116, v116\n"
    "v_mul_f32 v69, v117, v117\n"
    "v_mul_f32 v70, v118, v118\n"
    "v_mul_f32 v71, v119, v119\n"
    "v_mul_f32 v72, v120, v120\n"
    "v_mul_f32 v73, v121, v121\n"
    "v_mul_f32 v74, v122, v122\n"
    "v_mul_f32 v75, v123, v123\n"
    "v_mul_f32 v76, v124, v124\n"
    "v_mul_f32 v77, v125, v125\n"
    "v_mul_f32 v78, v126, v126\n"
    "v_mul_f32 v79, v127, v127\n"
    "v_fmaak_f32 v64, v64, v104, 0xc0135761\n"
    "v_fmaak_f32 v65, v65, v104, 0xc0135761\n"
    "v_fmaak_f32 v66, v66, v104, 0xc0135761\n"
    "v_fmaak_f32 v67, v67, v104, 0xc0135761\n"
    "v_fmaak_f32 v68, v68, v104, 0xc0135761\n"
    "v_fmaak_f32 v69, v69, v104, 0xc0135761\n"
    "v_fmaak_f32 v70, v70, v104, 0xc0135761\n"
    "v_fmaak_f32 v71, v71, v104, 0xc0135761\n"
    "v_fmaak_f32 v72, v72, v104, 0xc0135761\n"
    "v_fmaak_f32 v73, v73, v104, 0xc0135761\n"
    "v_fmaak_f32 v74, v74, v104, 0xc0135761\n"
    "v_fmaak_f32 v75, v75, v104, 0xc0135761\n"
    "v_fmaak_f32 v76, v76, v104, 0xc0135761\n"
    "v_fmaak_f32 v77, v77, v104, 0xc0135761\n"
    "v_fmaak_f32 v78, v78, v104, 0xc0135761\n"
    "v_fmaak_f32 v79, v79, v104, 0xc0135761\n"
    "v_mul_f32 v64, v64, v112\n"
    "v_mul_f32 v65, v65, v113\n"
    "v_mul_f32 v66, v66, v114\n"
    "v_mul_f32 v67, v67, v115\n"
    "v_mul_f32 v68, v68, v116\n"
    "v_mul_f32 v69, v69, v117\n"
    "v_mul_f32 v70, v70, v118\n"
    "v_mul_f32 v71, v71, v119\n"
    "v_mul_f32 v72, v72, v120\n"
    "v_mul_f32 v73, v73, v121\n"
    "v_mul_f32 v74, v74, v122\n"
    "v_mul_f32 v75, v75, v123\n"
    "v_mul_f32 v76, v76, v124\n"
    "v_mul_f32 v77, v77, v125\n"
    "v_mul_f32 v78, v78, v126\n"
    "v_mul_f32 v79, v79, v127\n"
    "v_exp_f32 v64, v64\n"
    "v_mul_f32 v112, v112, v100\n"
    "v_exp_f32 v65, v65\n"
    "v_mul_f32 v113, v113, v101\n"
    "v_exp_f32 v66, v66\n"
    "v_mul_f32 v114, v114, v102\n"
    "v_exp_f32 v67, v67\n"
    "v_mul_f32 v115, v115, v103\n"
    "v_exp_f32 v68, v68\n"
    "v_mul_f32 v116, v116, v100\n"
    "v_exp_f32 v69, v69\n"
    "v_mul_f32 v117, v117, v101\n"
    "v_exp_f32 v70, v70\n"
    "v_mul_f32 v118, v118, v102\n"
    "v_exp_f32 v71, v71\n"
    "v_mul_f32 v119, v119, v103\n"
    "ds_read_b128 v[128:131], v0 offset:65024\n"
    "v_exp_f32 v72, v72\n"
    "v_mul_f32 v120, v120, v100\n"
    "v_exp_f32 v73, v73\n"
    "v_mul_f32 v121, v121, v101\n"
    "v_exp_f32 v74, v74\n"
    "v_mul_f32 v122, v122, v102\n"
    "ds_read_b128 v[132:135], v1 offset:65024\n"
    "v_exp_f32 v75, v75\n"
    "v_mul_f32 v123, v123, v103\n"
    "v_exp_f32 v76, v76\n"
    "v_mul_f32 v124, v124, v100\n"
    "v_exp_f32 v77, v77\n"
    "v_mul_f32 v125, v125, v101\n"
    "ds_read_b128 v[136:139], v0\n"
    "v_exp_f32 v78, v78\n"
    "v_mul_f32 v126, v126, v102\n"
    "v_exp_f32 v79, v79\n"
    "v_mul_f32 v127, v127, v103\n"
    "v_add_f32 v64, 1.0, v64\n"
    "v_add_f32 v65, 1.0, v65\n"
    "ds_read_b128 v[140:143], v1\n"
    "v_add_f32 v66, 1.0, v66\n"
    "v_add_f32 v67, 1.0, v67\n"
    "v_add_f32 v68, 1.0, v68\n"
    "v_add_f32 v69, 1.0, v69\n"
    "v_add_f32 v70, 1.0, v70\n"
    "v_add_f32 v71, 1.0, v71\n"
    "ds_read_b128 v[144:147], v2 offset:65024\n"
    "v_add_f32 v72, 1.0, v72\n"
    "v_add_f32 v73, 1.0, v73\n"
    "v_add_f32 v74, 1.0, v74\n"
    "v_add_f32 v75, 1.0, v75\n"
    "v_add_f32 v76, 1.0, v76\n"
    "v_add_f32 v77, 1.0, v77\n"
    "ds_read_b128 v[148:151], v3 offset:65024\n"
    "v_add_f32 v78, 1.0, v78\n"
    "v_add_f32 v79, 1.0, v79\n"
    "v_rcp_f32 v64, v64\n"
    "v_rcp_f32 v65, v65\n"
    "v_rcp_f32 v66, v66\n"
    "v_rcp_f32 v67, v67\n"
    "ds_read_b128 v[152:155], v2\n"
    "v_rcp_f32 v68, v68\n"
    "v_rcp_f32 v69, v69\n"
    "v_rcp_f32 v70, v70\n"
    "v_rcp_f32 v71, v71\n"
    "v_rcp_f32 v72, v72\n"
    "v_rcp_f32 v73, v73\n"
    "ds_read_b128 v[156:159], v3\n"
    "v_rcp_f32 v74, v74\n"
    "v_rcp_f32 v75, v75\n"
    "v_rcp_f32 v76, v76\n"
    "v_rcp_f32 v77, v77\n"
    "v_rcp_f32 v78, v78\n"
    "v_rcp_f32 v79, v79\n"
    "ds_read_b128 v[160:163], v4 offset:65024\n"
    "v_mov_b32 v81, s12\n"
    "v_mov_b32 v83, s13\n"
    "v_mul_f32 v80, v64, v112\n"
    "v_mul_f32 v82, v68, v116\n"
    "v_mul_f32 v84, v72, v120\n"
    "v_mul_f32 v85, v76, v124\n"
    "ds_read_b128 v[164:167], v5 offset:65024\n"
    "v_fmac_f32 v80, v65, v113\n"
    "v_fmac_f32 v82, v69, v117\n"
    "v_fmac_f32 v84, v73, v121\n"
    "v_fmac_f32 v85, v77, v125\n"
    "v_fmac_f32 v80, v66, v114\n"
    "v_fmac_f32 v82, v70, v118\n"
    "ds_read_b128 v[168:171], v4\n"
    "v_fmac_f32 v84, v74, v122\n"
    "v_fmac_f32 v85, v78, v126\n"
    "v_fmac_f32 v80, v67, v115\n"
    "v_fmac_f32 v82, v71, v119\n"
    "v_fmac_f32 v84, v75, v123\n"
    "v_fmac_f32 v85, v79, v127\n"
    "ds_read_b128 v[172:175], v5\n"
    "v_permlane16_swap_b32 v80, v84\n"
    "v_permlane16_swap_b32 v82, v85\n"
    "v_add_f32 v80, v80, v84\n"
    "v_add_f32 v82, v82, v85\n"
    "buffer_store_dwordx4 v[80:83], v57, s[48:51], s62 offen sc1\n"
    "s_setprio 3\n"
    "s_waitcnt lgkmcnt(11)\n"
    "v_mfma_f32_16x16x32_bf16 v[16:19], v[128:131], v[128:131], 0\n"
    "ds_read_b32 v40, v58 offset:1536\n"
    "s_waitcnt lgkmcnt(10)\n"
    "v_mfma_f32_16x16x32_bf16 v[16:19], v[136:139], v[136:139], v[16:19]\n"
    "v_mfma_f32_16x16x32_bf16 v[20:23], v[132:135], v[132:135], 0\n"
    "s_waitcnt lgkmcnt(9)\n"
    "v_mfma_f32_16x16x32_bf16 v[20:23], v[140:143], v[140:143], v[20:23]\n"
    "s_waitcnt lgkmcnt(8)\n"
    "v_mfma_f32_16x16x32_bf16 v[24:27], v[128:131], v[144:147], 0\n"
    "ds_read_b32 v44, v58\n"
    "s_waitcnt lgkmcnt(7)\n"
    "v_mfma_f32_16x16x32_bf16 v[24:27], v[136:139], v[152:155], v[24:27]\n"
    "v_mfma_f32_16x16x32_bf16 v[28:31], v[132:135], v[148:151], 0\n"
    "s_waitcnt lgkmcnt(6)\n"
    "v_mfma_f32_16x16x32_bf16 v[28:31], v[140:143], v[156:159], v[28:31]\n"
    "v_mfma_f32_16x16x32_bf16 v[32:35], v[144:147], v[144:147], 0\n"
    "ds_read_b128 v[48:51], v56 offset:10368\n"
    "s_waitcnt lgkmcnt(2)\n"
    "v_mul_f32 v52, v16, v40\n"
    "v_mfma_f32_16x16x32_bf16 v[32:35], v[152:155], v[152:155], v[32:35]\n"
    "ds_read_b128 v[176:179], v6 offset:65024\n"
    "v_mfma_f32_16x16x32_bf16 v[36:39], v[148:151], v[148:151], 0\n"
    "v_mul_f32 v54, v20, v40\n"
    "v_mfma_f32_16x16x32_bf16 v[36:39], v[156:159], v[156:159], v[36:39]\n"
    "ds_read_b128 v[180:183], v7 offset:65024\n"
    "v_mfma_f32_16x16x32_bf16 v[16:19], v[128:131], v[160:163], 0\n"
    "ds_read_b32 v40, v58 offset:256\n"
    "s_waitcnt lgkmcnt(4)\n"
    "v_fmac_f32 v52, v24, v44\n"
    "v_mfma_f32_16x16x32_bf16 v[16:19], v[136:139], v[168:171], v[16:19]\n"
    "ds_read_b128 v[184:187], v6\n"
    "v_mfma_f32_16x16x32_bf16 v[20:23], v[132:135], v[164:167], 0\n"
    "v_fmac_f32 v54, v28, v44\n"
    "v_mfma_f32_16x16x32_bf16 v[20:23], v[140:143], v[172:175], v[20:23]\n"
    "ds_read_b128 v[188:191], v7\n"
    "v_mfma_f32_16x16x32_bf16 v[24:27], v[144:147], v[160:163], 0\n"
    "ds_read_b128 v[44:47], v56 offset:11392\n"
    "s_waitcnt lgkmcnt(6)\n"
    "v_fmac_f32 v52, v32, v48\n"
    "v_mul_f32 v53, v33, v49\n"
    "v_mfma_f32_16x16x32_bf16 v[24:27], v[152:155], v[168:171], v[24:27]\n"
    "v_fmac_f32 v52, v34, v50\n"
    "v_fmac_f32 v53, v35, v51\n"
    "v_mfma_f32_16x16x32_bf16 v[28:31], v[148:151], v[164:167], 0\n"
    "v_fmac_f32 v54, v36, v48\n"
    "v_mul_f32 v55, v37, v49\n"
    "v_mfma_f32_16x16x32_bf16 v[28:31], v[156:159], v[172:175], v[28:31]\n"
    "v_fmac_f32 v54, v38, v50\n"
    "v_fmac_f32 v55, v39, v51\n"
    "v_mfma_f32_16x16x32_bf16 v[32:35], v[160:163], v[160:163], 0\n"
    "ds_read_b128 v[48:51], v56 offset:12416\n"
    "s_waitcnt lgkmcnt(4)\n"
    "v_fmac_f32 v52, v16, v40\n"
    "v_mfma_f32_16x16x32_bf16 v[32:35], v[168:171], v[168:171], v[32:35]\n"
    "v_mfma_f32_16x16x32_bf16 v[36:39], v[164:167], v[164:167], 0\n"
    "v_fmac_f32 v54, v20, v40\n"
    "v_mfma_f32_16x16x32_bf16 v[36:39], v[172:175], v[172:175], v[36:39]\n"
    "v_mfma_f32_16x16x32_bf16 v[16:19], v[128:131], v[176:179], 0\n"
    "ds_read_b32 v40, v58 offset:512\n"
    "s_waitcnt lgkmcnt(2)\n"
    "v_fmac_f32 v52, v24, v44\n"
    "v_fmac_f32 v53, v25, v45\n"
    "v_mfma_f32_16x16x32_bf16 v[16:19], v[136:139], v[184:187], v[16:19]\n"
    "v_fmac_f32 v52, v26, v46\n"
    "v_fmac_f32 v53, v27, v47\n"
    "ds_read_b128 v[192:195], v8 offset:65024\n"
    "v_mfma_f32_16x16x32_bf16 v[20:23], v[132:135], v[180:183], 0\n"
    "v_fmac_f32 v54, v28, v44\n"
    "v_fmac_f32 v55, v29, v45\n"
    "v_mfma_f32_16x16x32_bf16 v[20:23], v[140:143], v[188:191], v[20:23]\n"
    "v_fmac_f32 v54, v30, v46\n"
    "v_fmac_f32 v55, v31, v47\n"
    "ds_read_b128 v[196:199], v9 offset:65024\n"
    "s_setprio 2\n"
    "v_mfma_f32_16x16x32_bf16 v[24:27], v[144:147], v[176:179], 0\n"
    "ds_read_b128 v[44:47], v56 offset:13440\n"
    "s_waitcnt lgkmcnt(4)\n"
    "v_fmac_f32 v52, v32, v48\n"
    "v_fmac_f32 v53, v33, v49\n"
    "v_mfma_f32_16x16x32_bf16 v[24:27], v[152:155], v[184:187], v[24:27]\n"
    "v_fmac_f32 v52, v34, v50\n"
    "v_fmac_f32 v53, v35, v51\n"
    "ds_read_b128 v[200:203], v8\n"
    "v_mfma_f32_16x16x32_bf16 v[28:31], v[148:151], v[180:183], 0\n"
    "v_fmac_f32 v54, v36, v48\n"
    "v_fmac_f32 v55, v37, v49\n"
    "v_mfma_f32_16x16x32_bf16 v[28:31], v[156:159], v[188:191], v[28:31]\n"
    "v_fmac_f32 v54, v38, v50\n"
    "v_fmac_f32 v55, v39, v51\n"
    "ds_read_b128 v[204:207], v9\n"
    "v_mfma_f32_16x16x32_bf16 v[32:35], v[160:163], v[176:179], 0\n"
    "ds_read_b128 v[48:51], v56 offset:14464\n"
    "s_waitcnt lgkmcnt(6)\n"
    "v_fmac_f32 v52, v16, v40\n"
    "v_mfma_f32_16x16x32_bf16 v[32:35], v[168:171], v[184:187], v[32:35]\n"
    "v_mfma_f32_16x16x32_bf16 v[36:39], v[164:167], v[180:183], 0\n"
    "v_fmac_f32 v54, v20, v40\n"
    "v_mfma_f32_16x16x32_bf16 v[36:39], v[172:175], v[188:191], v[36:39]\n"
    "v_mfma_f32_16x16x32_bf16 v[16:19], v[176:179], v[176:179], 0\n"
    "ds_read_b128 v[40:43], v56 offset:15488\n"
    "s_waitcnt lgkmcnt(4)\n"
    "v_fmac_f32 v52, v24, v44\n"
    "v_fmac_f32 v53, v25, v45\n"
    "v_mfma_f32_16x16x32_bf16 v[16:19], v[184:187], v[184:187], v[16:19]\n"
    "v_fmac_f32 v52, v26, v46\n"
    "v_fmac_f32 v53, v27, v47\n"
    "v_mfma_f32_16x16x32_bf16 v[20:23], v[180:183], v[180:183], 0\n"
    "v_fmac_f32 v54, v28, v44\n"
    "v_fmac_f32 v55, v29, v45\n"
    "v_mfma_f32_16x16x32_bf16 v[20:23], v[188:191], v[188:191], v[20:23]\n"
    "v_fmac_f32 v54, v30, v46\n"
    "v_fmac_f32 v55, v31, v47\n"
    "v_mfma_f32_16x16x32_bf16 v[24:27], v[128:131], v[192:195], 0\n"
    "ds_read_b32 v44, v58 offset:768\n"
    "s_waitcnt lgkmcnt(2)\n"
    "v_fmac_f32 v52, v32, v48\n"
    "v_fmac_f32 v53, v33, v49\n"
    "v_mfma_f32_16x16x32_bf16 v[24:27], v[136:139], v[200:203], v[24:27]\n"
    "v_fmac_f32 v52, v34, v50\n"
    "v_fmac_f32 v53, v35, v51\n"
    "v_mfma_f32_16x16x32_bf16 v[28:31], v[132:135], v[196:199], 0\n"
    "v_fmac_f32 v54, v36, v48\n"
    "v_fmac_f32 v55, v37, v49\n"
    "v_mfma_f32_16x16x32_bf16 v[28:31], v[140:143], v[204:207], v[28:31]\n"
    "v_fmac_f32 v54, v38, v50\n"
    "v_fmac_f32 v55, v39, v51\n"
    "v_mfma_f32_16x16x32_bf16 v[32:35], v[144:147], v[192:195], 0\n"
    "ds_read_b128 v[48:51], v56 offset:16640\n"
    "s_waitcnt lgkmcnt(2)\n"
    "v_fmac_f32 v52, v16, v40\n"
    "v_fmac_f32 v53, v17, v41\n"
    "v_mfma_f32_16x16x32_bf16 v[32:35], v[152:155], v[200:203], v[32:35]\n"
    "v_fmac_f32 v52, v18, v42\n"
    "v_fmac_f32 v53, v19, v43\n"
    "ds_read_b128 v[208:211], v10 offset:65024\n"
    "v_mfma_f32_16x16x32_bf16 v[36:39], v[148:151], v[196:199], 0\n"
    "v_fmac_f32 v54, v20, v40\n"
    "v_fmac_f32 v55, v21, v41\n"
    "v_mfma_f32_16x16x32_bf16 v[36:39], v[156:159], v[204:207], v[36:39]\n"
    "v_fmac_f32 v54, v22, v42\n"
    "v_fmac_f32 v55, v23, v43\n"
    "ds_read_b128 v[212:215], v11 offset:65024\n"
    "v_mfma_f32_16x16x32_bf16 v[16:19], v[160:163], v[192:195], 0\n"
    "ds_read_b128 v[40:43], v56 offset:17664\n"
    "s_waitcnt lgkmcnt(4)\n"
    "v_fmac_f32 v52, v24, v44\n"
    "v_mfma_f32_16x16x32_bf16 v[16:19], v[168:171], v[200:203], v[16:19]\n"
    "ds_read_b128 v[216:219], v10\n"
    "v_mfma_f32_16x16x32_bf16 v[20:23], v[164:167], v[196:199], 0\n"
    "v_fmac_f32 v54, v28, v44\n"
    "v_mfma_f32_16x16x32_bf16 v[20:23], v[172:175], v[204:207], v[20:23]\n"
    "ds_read_b128 v[220:223], v11\n"
    "v_mfma_f32_16x16x32_bf16 v[24:27], v[176:179], v[192:195], 0\n"
    "ds_read_b128 v[44:47], v56 offset:18688\n"
    "s_waitcnt lgkmcnt(6)\n"
    "v_fmac_f32 v52, v32, v48\n"
    "v_fmac_f32 v53, v33, v49\n"
    "v_mfma_f32_16x16x32_bf16 v[24:27], v[184:187], v[200:203], v[24:27]\n"
    "v_fmac_f32 v52, v34, v50\n"
    "v_fmac_f32 v53, v35, v51\n"
    "v_mfma_f32_16x16x32_bf16 v[28:31], v[180:183], v[196:199], 0\n"
    "v_fmac_f32 v54, v36, v48\n"
    "v_fmac_f32 v55, v37, v49\n"
    "v_mfma_f32_16x16x32_bf16 v[28:31], v[188:191], v[204:207], v[28:31]\n"
    "v_fmac_f32 v54, v38, v50\n"
    "v_fmac_f32 v55, v39, v51\n"
    "s_setprio 1\n"
    "v_mfma_f32_16x16x32_bf16 v[32:35], v[192:195], v[192:195], 0\n"
    "ds_read_b128 v[48:51], v56 offset:19712\n"
    "s_waitcnt lgkmcnt(4)\n"
    "v_fmac_f32 v52, v16, v40\n"
    "v_fmac_f32 v53, v17, v41\n"
    "v_mfma_f32_16x16x32_bf16 v[32:35], v[200:203], v[200:203], v[32:35]\n"
    "v_fmac_f32 v52, v18, v42\n"
    "v_fmac_f32 v53, v19, v43\n"
    "v_mfma_f32_16x16x32_bf16 v[36:39], v[196:199], v[196:199], 0\n"
    "v_fmac_f32 v54, v20, v40\n"
    "v_fmac_f32 v55, v21, v41\n"
    "v_mfma_f32_16x16x32_bf16 v[36:39], v[204:207], v[204:207], v[36:39]\n"
    "v_fmac_f32 v54, v22, v42\n"
    "v_fmac_f32 v55, v23, v43\n"
    "v_mfma_f32_16x16x32_bf16 v[16:19], v[144:147], v[208:211], 0\n"
    "ds_read_b128 v[40:43], v56 offset:20736\n"
    "s_waitcnt lgkmcnt(2)\n"
    "v_fmac_f32 v52, v24, v44\n"
    "v_fmac_f32 v53, v25, v45\n"
    "v_mfma_f32_16x16x32_bf16 v[16:19], v[152:155], v[216:219], v[16:19]\n"
    "v_fmac_f32 v52, v26, v46\n"
    "v_fmac_f32 v53, v27, v47\n"
    "v_mfma_f32_16x16x32_bf16 v[20:23], v[148:151], v[212:215], 0\n"
    "v_fmac_f32 v54, v28, v44\n"
    "v_fmac_f32 v55, v29, v45\n"
    "v_mfma_f32_16x16x32_bf16 v[20:23], v[156:159], v[220:223], v[20:23]\n"
    "v_fmac_f32 v54, v30, v46\n"
    "v_fmac_f32 v55, v31, v47\n"
    "v_mfma_f32_16x16x32_bf16 v[24:27], v[160:163], v[208:211], 0\n"
    "ds_read_b128 v[44:47], v56 offset:21760\n"
    "s_waitcnt lgkmcnt(2)\n"
    "v_fmac_f32 v52, v32, v48\n"
    "v_fmac_f32 v53, v33, v49\n"
    "v_mfma_f32_16x16x32_bf16 v[24:27], v[168:171], v[216:219], v[24:27]\n"
    "v_fmac_f32 v52, v34, v50\n"
    "v_fmac_f32 v53, v35, v51\n"
    "v_mfma_f32_16x16x32_bf16 v[28:31], v[164:167], v[212:215], 0\n"
    "v_fmac_f32 v54, v36, v48\n"
    "v_fmac_f32 v55, v37, v49\n"
    "v_mfma_f32_16x16x32_bf16 v[28:31], v[172:175], v[220:223], v[28:31]\n"
    "v_fmac_f32 v54, v38, v50\n"
    "v_fmac_f32 v55, v39, v51\n"
    "v_mfma_f32_16x16x32_bf16 v[32:35], v[176:179], v[208:211], 0\n"
    "ds_read_b128 v[48:51], v56 offset:22784\n"
    "s_waitcnt lgkmcnt(2)\n"
    "v_fmac_f32 v52, v16, v40\n"
    "v_fmac_f32 v53, v17, v41\n"
    "v_mfma_f32_16x16x32_bf16 v[32:35], v[184:187], v[216:219], v[32:35]\n"
    "v_fmac_f32 v52, v18, v42\n"
    "v_fmac_f32 v53, v19, v43\n"
    "ds_read_b128 v[224:227], v12 offset:65024\n"
    "v_mfma_f32_16x16x32_bf16 v[36:39], v[180:183], v[212:215], 0\n"
    "v_fmac_f32 v54, v20, v40\n"
    "v_fmac_f32 v55, v21, v41\n"
    "v_mfma_f32_16x16x32_bf16 v[36:39], v[188:191], v[220:223], v[36:39]\n"
    "v_fmac_f32 v54, v22, v42\n"
    "v_fmac_f32 v55, v23, v43\n"
    "ds_read_b128 v[228:231], v13 offset:65024\n"
    "buffer_load_dwordx4 v[92:95], v108, s[48:51], s63 offen sc1\n"
    "v_add_f32 v111, s98, v111\n"
    "v_mfma_f32_16x16x32_bf16 v[16:19], v[192:195], v[208:211], 0\n"
    "ds_read_b128 v[40:43], v56 offset:23808\n"
    "s_waitcnt lgkmcnt(4)\n"
    "v_fmac_f32 v52, v24, v44\n"
    "v_fmac_f32 v53, v25, v45\n"
    "v_mfma_f32_16x16x32_bf16 v[16:19], v[200:203], v[216:219], v[16:19]\n"
    "v_fmac_f32 v52, v26, v46\n"
    "v_fmac_f32 v53, v27, v47\n"
    "ds_read_b128 v[232:235], v12\n"
    "v_mfma_f32_16x16x32_bf16 v[20:23], v[196:199], v[212:215], 0\n"
    "v_fmac_f32 v54, v28, v44\n"
    "v_fmac_f32 v55, v29, v45\n"
    "v_mfma_f32_16x16x32_bf16 v[20:23], v[204:207], v[220:223], v[20:23]\n"
    "v_fmac_f32 v54, v30, v46\n"
    "v_fmac_f32 v55, v31, v47\n"
    "ds_read_b128 v[236:239], v13\n"
    "v_mfma_f32_16x16x32_bf16 v[24:27], v[208:211], v[208:211], 0\n"
    "ds_read_b128 v[44:47], v56\n"
    "s_waitcnt lgkmcnt(6)\n"
    "v_fmac_f32 v52, v32, v48\n"
    "v_fmac_f32 v53, v33, v49\n"
    "v_mfma_f32_16x16x32_bf16 v[24:27], v[216:219], v[216:219], v[24:27]\n"
    "v_fmac_f32 v52, v34, v50\n"
    "v_fmac_f32 v53, v35, v51\n"
    "v_mfma_f32_16x16x32_bf16 v[28:31], v[212:215], v[212:215], 0\n"
    "v_fmac_f32 v54, v36, v48\n"
    "v_fmac_f32 v55, v37, v49\n"
    "v_mfma_f32_16x16x32_bf16 v[28:31], v[220:223], v[220:223], v[28:31]\n"
    "v_fmac_f32 v54, v38, v50\n"
    "v_fmac_f32 v55, v39, v51\n"
    "s_waitcnt lgkmcnt(5)\n"
    "v_mfma_f32_16x16x32_bf16 v[32:35], v[144:147], v[224:227], 0\n"
    "ds_read_b128 v[48:51], v56 offset:1024\n"
    "s_waitcnt lgkmcnt(4)\n"
    "v_fmac_f32 v52, v16, v40\n"
    "v_fmac_f32 v53, v17, v41\n"
    "s_waitcnt lgkmcnt(3)\n"
    "v_mfma_f32_16x16x32_bf16 v[32:35], v[152:155], v[232:235], v[32:35]\n"
    "v_fmac_f32 v52, v18, v42\n"
    "v_fmac_f32 v53, v19, v43\n"
    "v_mfma_f32_16x16x32_bf16 v[36:39], v[148:151], v[228:231], 0\n"
    "v_fmac_f32 v54, v20, v40\n"
    "v_fmac_f32 v55, v21, v41\n"
    "s_waitcnt lgkmcnt(2)\n"
    "v_mfma_f32_16x16x32_bf16 v[36:39], v[156:159], v[236:239], v[36:39]\n"
    "v_fmac_f32 v54, v22, v42\n"
    "v_fmac_f32 v55, v23, v43\n"
    "s_setprio 0\n"
    "v_mfma_f32_16x16x32_bf16 v[16:19], v[160:163], v[224:227], 0\n"
    "ds_read_b128 v[40:43], v56 offset:2048\n"
    "s_waitcnt lgkmcnt(2)\n"
    "v_fmac_f32 v52, v24, v44\n"
    "v_fmac_f32 v53, v25, v45\n"
    "v_mfma_f32_16x16x32_bf16 v[16:19], v[168:171], v[232:235], v[16:19]\n"
    "v_fmac_f32 v52, v26, v46\n"
    "v_fmac_f32 v53, v27, v47\n"
    "v_mfma_f32_16x16x32_bf16 v[20:23], v[164:167], v[228:231], 0\n"
    "v_fmac_f32 v54, v28, v44\n"
    "v_fmac_f32 v55, v29, v45\n"
    "v_mfma_f32_16x16x32_bf16 v[20:23], v[172:175], v[236:239], v[20:23]\n"
    "v_fmac_f32 v54, v30, v46\n"
    "v_fmac_f32 v55, v31, v47\n"
    "v_mfma_f32_16x16x32_bf16 v[24:27], v[176:179], v[224:227], 0\n"
    "ds_read_b128 v[44:47], v56 offset:3072\n"
    "s_waitcnt lgkmcnt(2)\n"
    "v_fmac_f32 v52, v32, v48\n"
    "v_fmac_f32 v53, v33, v49\n"
    "v_mfma_f32_16x16x32_bf16 v[24:27], v[184:187], v[232:235], v[24:27]\n"
    "v_fmac_f32 v52, v34, v50\n"
    "v_fmac_f32 v53, v35, v51\n"
    "v_mfma_f32_16x16x32_bf16 v[28:31], v[180:183], v[228:231], 0\n"
    "v_fmac_f32 v54, v36, v48\n"
    "v_fmac_f32 v55, v37, v49\n"
    "v_mfma_f32_16x16x32_bf16 v[28:31], v[188:191], v[236:239], v[28:31]\n"
    "v_fmac_f32 v54, v38, v50\n"
    "v_fmac_f32 v55, v39, v51\n"
    "v_mfma_f32_16x16x32_bf16 v[32:35], v[192:195], v[224:227], 0\n"
    "ds_read_b128 v[48:51], v56 offset:4096\n"
    "s_waitcnt lgkmcnt(2)\n"
    "v_fmac_f32 v52, v16, v40\n"
    "v_fmac_f32 v53, v17, v41\n"
    "v_mfma_f32_16x16x32_bf16 v[32:35], v[200:203], v[232:235], v[32:35]\n"
    "v_fmac_f32 v52, v18, v42\n"
    "v_fmac_f32 v53, v19, v43\n"
    "v_mfma_f32_16x16x32_bf16 v[36:39], v[196:199], v[228:231], 0\n"
    "v_fmac_f32 v54, v20, v40\n"
    "v_fmac_f32 v55, v21, v41\n"
    "v_mfma_f32_16x16x32_bf16 v[36:39], v[204:207], v[236:239], v[36:39]\n"
    "v_fmac_f32 v54, v22, v42\n"
    "v_fmac_f32 v55, v23, v43\n"
    "v_mfma_f32_16x16x32_bf16 v[16:19], v[208:211], v[224:227], 0\n"
    "ds_read_b128 v[40:43], v56 offset:5120\n"
    "s_waitcnt lgkmcnt(2)\n"
    "v_fmac_f32 v52, v24, v44\n"
    "v_fmac_f32 v53, v25, v45\n"
    "v_mfma_f32_16x16x32_bf16 v[16:19], v[216:219], v[232:235], v[16:19]\n"
    "v_fmac_f32 v52, v26, v46\n"
    "v_fmac_f32 v53, v27, v47\n"
    "v_mfma_f32_16x16x32_bf16 v[20:23], v[212:215], v[228:231], 0\n"
    "v_fmac_f32 v54, v28, v44\n"
    "v_fmac_f32 v55, v29, v45\n"
    "v_mfma_f32_16x16x32_bf16 v[20:23], v[220:223], v[236:239], v[20:23]\n"
    "v_fmac_f32 v54, v30, v46\n"
    "v_fmac_f32 v55, v31, v47\n"
    "v_mfma_f32_16x16x32_bf16 v[24:27], v[224:227], v[224:227], 0\n"
    "ds_read_b128 v[44:47], v56 offset:6144\n"
    "s_waitcnt lgkmcnt(2)\n"
    "v_fmac_f32 v52, v32, v48\n"
    "v_fmac_f32 v53, v33, v49\n"
    "v_mfma_f32_16x16x32_bf16 v[24:27], v[232:235], v[232:235], v[24:27]\n"
    "v_fmac_f32 v52, v34, v50\n"
    "v_fmac_f32 v53, v35, v51\n"
    "v_mfma_f32_16x16x32_bf16 v[28:31], v[228:231], v[228:231], 0\n"
    "v_fmac_f32 v54, v36, v48\n"
    "v_fmac_f32 v55, v37, v49\n"
    "v_mfma_f32_16x16x32_bf16 v[28:31], v[236:239], v[236:239], v[28:31]\n"
    "v_fmac_f32 v54, v38, v50\n"
    "v_fmac_f32 v55, v39, v51\n"
    "v_mfma_f32_16x16x32_bf16 v[32:35], v[128:131], v[208:211], 0\n"
    "ds_read_b32 v48, v58 offset:1024\n"
    "s_waitcnt lgkmcnt(2)\n"
    "v_fmac_f32 v52, v16, v40\n"
    "v_fmac_f32 v53, v17, v41\n"
    "v_mfma_f32_16x16x32_bf16 v[32:35], v[136:139], v[216:219], v[32:35]\n"
    "v_fmac_f32 v52, v18, v42\n"
    "v_fmac_f32 v53, v19, v43\n"
    "v_mfma_f32_16x16x32_bf16 v[36:39], v[132:135], v[212:215], 0\n"
    "v_fmac_f32 v54, v20, v40\n"
    "v_fmac_f32 v55, v21, v41\n"
    "v_mfma_f32_16x16x32_bf16 v[36:39], v[140:143], v[220:223], v[36:39]\n"
    "v_fmac_f32 v54, v22, v42\n"
    "v_fmac_f32 v55, v23, v43\n"
    "v_mfma_f32_16x16x32_bf16 v[16:19], v[128:131], v[224:227], 0\n"
    "ds_read_b32 v40, v58 offset:1280\n"
    "s_waitcnt lgkmcnt(2)\n"
    "v_fmac_f32 v52, v24, v44\n"
    "v_fmac_f32 v53, v25, v45\n"
    "v_mfma_f32_16x16x32_bf16 v[16:19], v[136:139], v[232:235], v[16:19]\n"
    "v_fmac_f32 v52, v26, v46\n"
    "v_fmac_f32 v53, v27, v47\n"
    "v_mfma_f32_16x16x32_bf16 v[20:23], v[132:135], v[228:231], 0\n"
    "v_fmac_f32 v54, v28, v44\n"
    "v_fmac_f32 v55, v29, v45\n"
    "v_mfma_f32_16x16x32_bf16 v[20:23], v[140:143], v[236:239], v[20:23]\n"
    "v_fmac_f32 v54, v30, v46\n"
    "v_fmac_f32 v55, v31, v47\n"
    "s_waitcnt vmcnt(0)\n"
    "v_cmp_ne_u32_e64 vcc, s12, v93\n"
    "v_cmp_ne_u32_e64 s[14:15], s13, v95\n"
    "s_waitcnt lgkmcnt(1)\n"
    "v_fmac_f32 v52, v32, v48\n"
    "v_fmac_f32 v54, v36, v48\n"
    "v_add_f32 v52, v52, v53\n"
    "v_add_f32 v54, v54, v55\n"
    "s_or_b64 s[14:15], s[14:15], vcc\n"
    "s_cbranch_scc1 L_p3aB_%=\n"
    "L_p3bB_%=:\n"
    "v_add_f32 v52, v92, v52\n"
    "v_add_f32 v54, v94, v54\n"
    "s_waitcnt lgkmcnt(0)\n"
    "v_fmac_f32 v52, v16, v40\n"
    "v_fmac_f32 v54, v20, v40\n"
    "s_nop 1\n"
    "v_permlane32_swap_b32 v52, v54\n"
    "v_add_f32 v52, v52, v54\n"
    "s_nop 1\n"
    "v_add_f32_dpp v52, v52, v52 row_ror:8 row_mask:0xf bank_mask:0xf\n"
    "s_nop 1\n"
    "v_add_f32_dpp v52, v52, v52 row_ror:4 row_mask:0xf bank_mask:0xf\n"
    "s_nop 1\n"
    "v_add_f32_dpp v52, v52, v52 row_ror:2 row_mask:0xf bank_mask:0xf\n"
    "s_nop 1\n"
    "v_add_f32_dpp v52, v52, v52 row_ror:1 row_mask:0xf bank_mask:0xf\n"
    "v_add_f32 v111, v52, v111\n"
    "s_mov_b32 s16, 0x10000\n"
    "s_mov_b32 s17, s16\n"
    "v_add_f32_dpp v111, v52, v111 row_bcast:15 row_mask:0xa bank_mask:0xf\n"
    "s_mov_b64 exec, s[16:17]\n"
    "v_cvt_pk_bf16_f32 v52, v111, v111\n"
    "global_store_short v110, v52, s[28:29]\n"
    "s_endpgm\n"
    "L_p3aB_%=:\n"
    "s_mov_b32 s66, 65536\n"
    "L_p3cB_%=:\n"
    "s_sleep 2\n"
    "s_mov_b64 exec, s[14:15]\n"
    "buffer_load_dwordx4 v[92:95], v108, s[48:51], s63 offen sc1\n"
    "s_mov_b64 exec, -1\n"
    "s_waitcnt vmcnt(0)\n"
    "v_cmp_ne_u32_e64 vcc, s12, v93\n"
    "v_cmp_ne_u32_e64 s[14:15], s13, v95\n"
    "s_nop 3\n"
    "s_or_b64 s[14:15], s[14:15], vcc\n"
    "s_cbranch_scc0 L_p3bB_%=\n"
    "s_sub_u32 s66, s66, 1\n"
    "s_cmp_lg_u32 s66, 0\n"
    "s_cbranch_scc1 L_p3cB_%=\n"
    "s_branch L_p3bB_%=\n"
    "L_GLA_%=:\n"
    "s_waitcnt lgkmcnt(0)\n"
    "s_barrier\n"
    "buffer_load_dwordx4 v[32:35], v57, s[48:51], s56 offen offset:2048 sc1\n"
    "buffer_load_dwordx4 v[36:39], v57, s[48:51], s56 offen offset:3072 sc1\n"
    "s_setprio 0\n"
    "s_waitcnt vmcnt(3)\n"
    "v_and_b32 v0, s43, v48\n"
    "v_lshrrev_b32 v4, 16, v48\n"
    "ds_read_b128 v[0:3], v0 offset:65520\n"
    "ds_read_b128 v[4:7], v4 offset:65520\n"
    "v_and_b32 v8, s43, v49\n"
    "v_lshrrev_b32 v12, 16, v49\n"
    "ds_read_b128 v[8:11], v8 offset:65520\n"
    "ds_read_b128 v[12:15], v12 offset:65520\n"
    "s_add_u32 s34, s34, s35\n"
    "s_lshl_b32 s35, s35, 1\n"
    "s_add_u32 s34, s34, 16384\n"
    "v_and_b32 v16, s43, v50\n"
    "v_lshrrev_b32 v20, 16, v50\n"
    "ds_read_b128 v[16:19], v16 offset:65520\n"
    "ds_read_b128 v[20:23], v20 offset:65520\n"
    "v_and_b32 v24, s43, v51\n"
    "v_lshrrev_b32 v28, 16, v51\n"
    "ds_read_b128 v[24:27], v24 offset:65520\n"
    "ds_read_b128 v[28:31], v28 offset:65520\n"
    "s_waitcnt vmcnt(2)\n"
    "v_and_b32 v44, s43, v52\n"
    "v_lshrrev_b32 v40, 16, v52\n"
    "s_waitcnt lgkmcnt(6)\n"
    "s_setprio 2\n"
    "v_smfmac_f32_16x16x64_bf16 v[64:67], v[60:63], v[0:7], v59\n"
    "s_setprio 0\n"
    "ds_read_b128 v[0:3], v44 offset:65520\n"
    "ds_read_b128 v[4:7], v40 offset:65520\n"
    "s_add_u32 s56, s56, 32768\n"
    "buffer_load_dwordx4 v[48:51], v57, s[48:51], s56 offen sc1\n"
    "v_and_b32 v45, s43, v53\n"
    "v_lshrrev_b32 v41, 16, v53\n"
    "s_waitcnt lgkmcnt(6)\n"
    "s_setprio 2\n"
    "v_smfmac_f32_16x16x64_bf16 v[68:71], v[60:63], v[8:15], v59\n"
    "s_setprio 0\n"
    "ds_read_b128 v[8:11], v45 offset:65520\n"
    "ds_read_b128 v[12:15], v41 offset:65520\n"
    "v_and_b32 v46, s43, v54\n"
    "v_lshrrev_b32 v42, 16, v54\n"
    "s_waitcnt lgkmcnt(6)\n"
    "s_setprio 2\n"
    "v_smfmac_f32_16x16x64_bf16 v[72:75], v[60:63], v[16:23], v59\n"
    "s_setprio 0\n"
    "ds_read_b128 v[16:19], v46 offset:65520\n"
    "ds_read_b128 v[20:23], v42 offset:65520\n"
    "v_and_b32 v47, s43, v55\n"
    "v_lshrrev_b32 v43, 16, v55\n"
    "s_waitcnt lgkmcnt(6)\n"
    "s_setprio 2\n"
    "v_smfmac_f32_16x16x64_bf16 v[76:79], v[60:63], v[24:31], v59\n"
    "s_setprio 0\n"
    "v_cvt_pk_bf16_f32 v64, v64, v65\n"
    "v_cvt_pk_bf16_f32 v65, v68, v69\n"
    "v_cvt_pk_bf16_f32 v66, v66, v67\n"
    "v_cvt_pk_bf16_f32 v67, v70, v71\n"
    "v_min_u32 v64, -2, v64\n"
    "buffer_store_dwordx4 v[64:67], v57, s[48:51], s18 offen sc1\n"
    "ds_read_b128 v[24:27], v47 offset:65520\n"
    "ds_read_b128 v[28:31], v43 offset:65520\n"
    "s_waitcnt vmcnt(3)\n"
    "v_and_b32 v44, s43, v32\n"
    "v_lshrrev_b32 v40, 16, v32\n"
    "s_waitcnt lgkmcnt(6)\n"
    "s_setprio 2\n"
    "v_smfmac_f32_16x16x64_bf16 v[80:83], v[60:63], v[0:7], v59\n"
    "s_setprio 0\n"
    "ds_read_b128 v[0:3], v44 offset:65520\n"
    "ds_read_b128 v[4:7], v40 offset:65520\n"
    "v_and_b32 v45, s43, v33\n"
    "v_lshrrev_b32 v41, 16, v33\n"
    "s_waitcnt lgkmcnt(6)\n"
    "s_setprio 2\n"
    "v_smfmac_f32_16x16x64_bf16 v[84:87], v[60:63], v[8:15], v59\n"
    "s_setprio 0\n"
    "v_cvt_pk_bf16_f32 v72, v72, v73\n"
    "v_cvt_pk_bf16_f32 v73, v76, v77\n"
    "v_cvt_pk_bf16_f32 v74, v74, v75\n"
    "v_cvt_pk_bf16_f32 v75, v78, v79\n"
    "v_min_u32 v72, -2, v72\n"
    "buffer_store_dwordx4 v[72:75], v57, s[48:51], s18 offen offset:1024 sc1\n"
    "ds_read_b128 v[8:11], v45 offset:65520\n"
    "ds_read_b128 v[12:15], v41 offset:65520\n"
    "v_and_b32 v46, s43, v34\n"
    "v_lshrrev_b32 v42, 16, v34\n"
    "s_waitcnt lgkmcnt(6)\n"
    "s_setprio 2\n"
    "v_smfmac_f32_16x16x64_bf16 v[88:91], v[60:63], v[16:23], v59\n"
    "s_setprio 0\n"
    "ds_read_b128 v[16:19], v46 offset:65520\n"
    "ds_read_b128 v[20:23], v42 offset:65520\n"
    "v_and_b32 v47, s43, v35\n"
    "v_lshrrev_b32 v43, 16, v35\n"
    "s_waitcnt lgkmcnt(6)\n"
    "s_setprio 2\n"
    "v_smfmac_f32_16x16x64_bf16 v[92:95], v[60:63], v[24:31], v59\n"
    "s_setprio 0\n"
    "v_cvt_pk_bf16_f32 v80, v80, v81\n"
    "v_cvt_pk_bf16_f32 v81, v84, v85\n"
    "v_cvt_pk_bf16_f32 v82, v82, v83\n"
    "v_cvt_pk_bf16_f32 v83, v86, v87\n"
    "v_min_u32 v80, -2, v80\n"
    "buffer_store_dwordx4 v[80:83], v57, s[48:51], s18 offen offset:2048 sc1\n"
    "ds_read_b128 v[24:27], v47 offset:65520\n"
    "ds_read_b128 v[28:31], v43 offset:65520\n"
    "s_waitcnt lgkmcnt(6)\n"
    "s_setprio 2\n"
    "v_smfmac_f32_16x16x64_bf16 v[96:99], v[60:63], v[0:7], v59\n"
    "s_setprio 0\n"
    "s_waitcnt lgkmcnt(4)\n"
    "s_setprio 2\n"
    "v_smfmac_f32_16x16x64_bf16 v[100:103], v[60:63], v[8:15], v59\n"
    "s_setprio 0\n"
    "v_cvt_pk_bf16_f32 v76, v88, v89\n"
    "v_cvt_pk_bf16_f32 v77, v92, v93\n"
    "v_cvt_pk_bf16_f32 v78, v90, v91\n"
    "v_cvt_pk_bf16_f32 v79, v94, v95\n"
    "v_min_u32 v76, -2, v76\n"
    "buffer_store_dwordx4 v[76:79], v57, s[48:51], s18 offen offset:3072 sc1\n"
    "s_waitcnt vmcnt(4)\n"
    "v_mov_b32 v52, v36\n"
    "v_mov_b32 v53, v37\n"
    "v_mov_b32 v54, v38\n"
    "v_mov_b32 v55, v39\n"
    "s_waitcnt lgkmcnt(2)\n"
    "s_setprio 2\n"
    "v_smfmac_f32_16x16x64_bf16 v[104:107], v[60:63], v[16:23], v59\n"
    "s_setprio 0\n"
    "s_waitcnt lgkmcnt(0)\n"
    "s_setprio 2\n"
    "v_smfmac_f32_16x16x64_bf16 v[108:111], v[60:63], v[24:31], v59\n"
    "s_setprio 0\n"
    "v_cvt_pk_bf16_f32 v68, v96, v97\n"
    "v_cvt_pk_bf16_f32 v69, v100, v101\n"
    "v_cvt_pk_bf16_f32 v70, v98, v99\n"
    "v_cvt_pk_bf16_f32 v71, v102, v103\n"
    "v_min_u32 v68, -2, v68\n"
    "buffer_store_dwordx4 v[68:71], v57, s[48:51], s19 offen sc1\n"
    "v_cvt_pk_bf16_f32 v92, v104, v105\n"
    "v_cvt_pk_bf16_f32 v93, v108, v109\n"
    "v_cvt_pk_bf16_f32 v94, v106, v107\n"
    "v_cvt_pk_bf16_f32 v95, v110, v111\n"
    "v_min_u32 v92, -2, v92\n"
    "buffer_store_dwordx4 v[92:95], v57, s[48:51], s19 offen offset:1024 sc1\n"
    "s_setprio 1\n"
    "ds_read_b64_tr_b16 v[0:1], v56\n"
    "ds_read_b64_tr_b16 v[2:3], v56 offset:64\n"
    "ds_read_b64_tr_b16 v[4:5], v56 offset:512\n"
    "ds_read_b64_tr_b16 v[6:7], v56 offset:576\n"
    "ds_read_b64_tr_b16 v[8:9], v56 offset:1024\n"
    "ds_read_b64_tr_b16 v[10:11], v56 offset:1088\n"
    "ds_read_b64_tr_b16 v[12:13], v56 offset:1536\n"
    "ds_read_b64_tr_b16 v[14:15], v56 offset:1600\n"
    "ds_read_b64_tr_b16 v[16:17], v56 offset:8\n"
    "ds_read_b64_tr_b16 v[18:19], v56 offset:72\n"
    "ds_read_b64_tr_b16 v[20:21], v56 offset:520\n"
    "ds_read_b64_tr_b16 v[22:23], v56 offset:584\n"
    "ds_read_b64_tr_b16 v[24:25], v56 offset:1032\n"
    "ds_read_b64_tr_b16 v[26:27], v56 offset:1096\n"
    "ds_read_b64_tr_b16 v[28:29], v56 offset:1544\n"
    "ds_read_b64_tr_b16 v[30:31], v56 offset:1608\n"
    "s_mov_b32 m0, s54\n"
    "s_waitcnt lgkmcnt(14)\n"
    "v_mfma_f32_16x16x32_bf16 v[32:35], v[128:131], v[0:3], 0\n"
    "s_waitcnt lgkmcnt(12)\n"
    "v_mfma_f32_16x16x32_bf16 v[32:35], v[132:135], v[4:7], v[32:35]\n"
    "s_waitcnt lgkmcnt(10)\n"
    "v_mfma_f32_16x16x32_bf16 v[32:35], v[136:139], v[8:11], v[32:35]\n"
    "s_waitcnt lgkmcnt(8)\n"
    "v_mfma_f32_16x16x32_bf16 v[32:35], v[140:143], v[12:15], v[32:35]\n"
    "s_waitcnt lgkmcnt(6)\n"
    "v_mfma_f32_16x16x32_bf16 v[36:39], v[128:131], v[16:19], 0\n"
    "s_lshl_b32 s65, s11, 22\n"
    "s_sub_u32 s57, s18, s65\n"
    "s_waitcnt lgkmcnt(4)\n"
    "v_mfma_f32_16x16x32_bf16 v[36:39], v[132:135], v[20:23], v[36:39]\n"
    "s_waitcnt lgkmcnt(2)\n"
    "v_mfma_f32_16x16x32_bf16 v[36:39], v[136:139], v[24:27], v[36:39]\n"
    "s_sub_u32 s65, s11, 1\n"
    "s_and_b32 s65, s65, 3\n"
    "s_lshl_b32 s65, s65, 22\n"
    "s_add_u32 s59, s65, s57\n"
    "s_waitcnt lgkmcnt(0)\n"
    "v_mfma_f32_16x16x32_bf16 v[36:39], v[140:143], v[28:31], v[36:39]\n"
    "v_mfma_f32_16x16x32_bf16 v[40:43], v[144:147], v[0:3], 0\n"
    "s_sub_u32 s65, s11, 2\n"
    "s_and_b32 s65, s65, 3\n"
    "s_lshl_b32 s65, s65, 22\n"
    "s_add_u32 s60, s65, s57\n"
    "s_add_u32 s60, s60, 2048\n"
    "v_mfma_f32_16x16x32_bf16 v[40:43], v[148:151], v[4:7], v[40:43]\n"
    "v_mfma_f32_16x16x32_bf16 v[40:43], v[152:155], v[8:11], v[40:43]\n"
    "s_sub_u32 s65, s11, 3\n"
    "s_and_b32 s65, s65, 3\n"
    "s_lshl_b32 s65, s65, 22\n"
    "s_add_u32 s61, s65, s57\n"
    "s_add_u32 s61, s61, 4096\n"
    "v_mfma_f32_16x16x32_bf16 v[40:43], v[156:159], v[12:15], v[40:43]\n"
    "v_mfma_f32_16x16x32_bf16 v[44:47], v[144:147], v[16:19], 0\n"
    "v_cvt_pk_bf16_f32 v36, v32, v36\n"
    "s_and_b32 s63, %[wg], 0xc0\n"
    "s_lshl_b32 s63, s63, 16\n"
    "s_lshl_b32 s65, s10, 4\n"
    "s_add_u32 s63, s63, s65\n"
    "v_mfma_f32_16x16x32_bf16 v[44:47], v[148:151], v[20:23], v[44:47]\n"
    "v_cvt_pk_bf16_f32 v37, v33, v37\n"
    "v_and_b32 v128, s43, v52\n"
    "v_lshrrev_b32 v132, 16, v52\n"
    "ds_read_b128 v[128:131], v128 offset:65520\n"
    "ds_read_b128 v[132:135], v132 offset:65520\n"
    "v_mfma_f32_16x16x32_bf16 v[44:47], v[152:155], v[24:27], v[44:47]\n"
    "v_cvt_pk_bf16_f32 v38, v34, v38\n"
    "s_and_b32 s65, %[wg], 0x38\n"
    "s_lshl_b32 s65, s65, 10\n"
    "s_add_u32 s63, s63, s65\n"
    "s_add_u32 s62, s19, 2048\n"
    "v_mfma_f32_16x16x32_bf16 v[44:47], v[156:159], v[28:31], v[44:47]\n"
    "v_cvt_pk_bf16_f32 v39, v35, v39\n"
    "v_mfma_f32_16x16x32_bf16 v[32:35], v[160:163], v[0:3], 0\n"
    "ds_write_addtid_b32 v36 offset:0\n"
    "s_and_b32 s65, %[wg], 7\n"
    "s_lshl_b32 s65, s65, 7\n"
    "s_add_u32 s63, s63, s65\n"
    "s_add_u32 s63, s63, 2103296\n"
    "v_mfma_f32_16x16x32_bf16 v[32:35], v[164:167], v[4:7], v[32:35]\n"
    "ds_write_addtid_b32 v37 offset:256\n"
    "v_mfma_f32_16x16x32_bf16 v[32:35], v[168:171], v[8:11], v[32:35]\n"
    "ds_write_addtid_b32 v38 offset:512\n"
    "s_lshr_b32 s53, %[wg], 1\n"
    "s_lshl_b32 s53, s53, 2\n"
    "s_and_b32 s65, %[wg], 1\n"
    "s_add_u32 s53, s53, s65\n"
    "s_lshl_b32 s53, s53, 3\n"
    "s_add_u32 s53, s53, s10\n"
    "s_lshl_b32 s53, s53, 1\n"
    "s_lshl_b32 s98, s98, 16\n"
    "v_mfma_f32_16x16x32_bf16 v[32:35], v[172:175], v[12:15], v[32:35]\n"
    "ds_write_addtid_b32 v39 offset:768\n"
    "v_mfma_f32_16x16x32_bf16 v[36:39], v[160:163], v[16:19], 0\n"
    "v_cvt_pk_bf16_f32 v44, v40, v44\n"
    "v_mfma_f32_16x16x32_bf16 v[36:39], v[164:167], v[20:23], v[36:39]\n"
    "v_cvt_pk_bf16_f32 v45, v41, v45\n"
    "v_mfma_f32_16x16x32_bf16 v[36:39], v[168:171], v[24:27], v[36:39]\n"
    "v_cvt_pk_bf16_f32 v46, v42, v46\n"
    "v_lshrrev_b32 v88, 8, v57\n"
    "v_mfma_f32_16x16x32_bf16 v[36:39], v[172:175], v[28:31], v[36:39]\n"
    "v_cvt_pk_bf16_f32 v47, v43, v47\n"
    "v_min_u32 v88, 2, v88\n"
    "v_mfma_f32_16x16x32_bf16 v[40:43], v[176:179], v[0:3], 0\n"
    "ds_write_addtid_b32 v44 offset:1024\n"
    "v_add3_u32 v88, v88, s11, 1\n"
    "v_mfma_f32_16x16x32_bf16 v[40:43], v[180:183], v[4:7], v[40:43]\n"
    "ds_write_addtid_b32 v45 offset:1280\n"
    "v_and_b32 v88, 3, v88\n"
    "v_mfma_f32_16x16x32_bf16 v[40:43], v[184:187], v[8:11], v[40:43]\n"
    "ds_write_addtid_b32 v46 offset:1536\n"
    "v_lshlrev_b32 v88, 22, v88\n"
    "v_mfma_f32_16x16x32_bf16 v[40:43], v[188:191], v[12:15], v[40:43]\n"
    "ds_write_addtid_b32 v47 offset:1792\n"
    "s_add_u32 s57, s57, 7168\n"
    "v_mfma_f32_16x16x32_bf16 v[44:47], v[176:179], v[16:19], 0\n"
    "v_cvt_pk_bf16_f32 v36, v32, v36\n"
    "v_and_b32 v160, s43, v53\n"
    "v_lshrrev_b32 v164, 16, v53\n"
    "ds_read_b128 v[160:163], v160 offset:65520\n"
    "ds_read_b128 v[164:167], v164 offset:65520\n"
    "v_mfma_f32_16x16x32_bf16 v[44:47], v[180:183], v[20:23], v[44:47]\n"
    "v_cvt_pk_bf16_f32 v37, v33, v37\n"
    "v_mfma_f32_16x16x32_bf16 v[44:47], v[184:187], v[24:27], v[44:47]\n"
    "v_cvt_pk_bf16_f32 v38, v34, v38\n"
    "v_mfma_f32_16x16x32_bf16 v[44:47], v[188:191], v[28:31], v[44:47]\n"
    "v_cvt_pk_bf16_f32 v39, v35, v39\n"
    "v_mfma_f32_16x16x32_bf16 v[32:35], v[192:195], v[0:3], 0\n"
    "ds_write_addtid_b32 v36 offset:2048\n"
    "v_mfma_f32_16x16x32_bf16 v[32:35], v[196:199], v[4:7], v[32:35]\n"
    "ds_write_addtid_b32 v37 offset:2304\n"
    "v_mfma_f32_16x16x32_bf16 v[32:35], v[200:203], v[8:11], v[32:35]\n"
    "ds_write_addtid_b32 v38 offset:2560\n"
    "v_mfma_f32_16x16x32_bf16 v[32:35], v[204:207], v[12:15], v[32:35]\n"
    "ds_write_addtid_b32 v39 offset:2816\n"
    "v_mfma_f32_16x16x32_bf16 v[36:39], v[192:195], v[16:19], 0\n"
    "v_cvt_pk_bf16_f32 v44, v40, v44\n"
    "v_mfma_f32_16x16x32_bf16 v[36:39], v[196:199], v[20:23], v[36:39]\n"
    "v_cvt_pk_bf16_f32 v45, v41, v45\n"
    "v_mfma_f32_16x16x32_bf16 v[36:39], v[200:203], v[24:27], v[36:39]\n"
    "v_cvt_pk_bf16_f32 v46, v42, v46\n"
    "v_mfma_f32_16x16x32_bf16 v[36:39], v[204:207], v[28:31], v[36:39]\n"
    "v_cvt_pk_bf16_f32 v47, v43, v47\n"
    "v_mfma_f32_16x16x32_bf16 v[40:43], v[208:211], v[0:3], 0\n"
    "ds_write_addtid_b32 v44 offset:3072\n"
    "s_mul_i32 s66, s10, 800\n"
    "s_add_u32 s66, s66, 156032\n"
    "v_mfma_f32_16x16x32_bf16 v[40:43], v[212:215], v[4:7], v[40:43]\n"
    "ds_write_addtid_b32 v45 offset:3328\n"
    "v_bfe_u32 v148, v57, 4, 4\n"
    "v_mfma_f32_16x16x32_bf16 v[40:43], v[216:219], v[8:11], v[40:43]\n"
    "ds_write_addtid_b32 v46 offset:3584\n"
    "v_and_b32 v149, 12, v148\n"
    "v_mfma_f32_16x16x32_bf16 v[40:43], v[220:223], v[12:15], v[40:43]\n"
    "ds_write_addtid_b32 v47 offset:3840\n"
    "v_lshl_add_u32 v148, v148, 2, s66\n"
    "v_mfma_f32_16x16x32_bf16 v[44:47], v[208:211], v[16:19], 0\n"
    "v_cvt_pk_bf16_f32 v36, v32, v36\n"
    "v_add_u32 v149, s66, v149\n"
    "v_mfma_f32_16x16x32_bf16 v[44:47], v[212:215], v[20:23], v[44:47]\n"
    "v_cvt_pk_bf16_f32 v37, v33, v37\n"
    "buffer_load_dwordx2 v[90:91], v88, s[48:51], s57 offen sc1\n"
    "v_mfma_f32_16x16x32_bf16 v[44:47], v[216:219], v[24:27], v[44:47]\n"
    "v_cvt_pk_bf16_f32 v38, v34, v38\n"
    "ds_read2_b32 v[176:177], v149 offset0:96 offset1:196\n"
    "v_mfma_f32_16x16x32_bf16 v[44:47], v[220:223], v[28:31], v[44:47]\n"
    "v_cvt_pk_bf16_f32 v39, v35, v39\n"
    "ds_read2_b32 v[178:179], v148 offset0:0 offset1:100\n"
    "v_mfma_f32_16x16x32_bf16 v[32:35], v[224:227], v[0:3], 0\n"
    "ds_write_addtid_b32 v36 offset:4096\n"
    "ds_read2_b32 v[180:181], v148 offset0:16 offset1:116\n"
    "v_mfma_f32_16x16x32_bf16 v[32:35], v[228:231], v[4:7], v[32:35]\n"
    "ds_write_addtid_b32 v37 offset:4352\n"
    "ds_read2_b32 v[182:183], v148 offset0:32 offset1:132\n"
    "v_mfma_f32_16x16x32_bf16 v[32:35], v[232:235], v[8:11], v[32:35]\n"
    "ds_write_addtid_b32 v38 offset:4608\n"
    "ds_read2_b32 v[192:193], v148 offset0:48 offset1:148\n"
    "v_mfma_f32_16x16x32_bf16 v[32:35], v[236:239], v[12:15], v[32:35]\n"
    "ds_write_addtid_b32 v39 offset:4864\n"
    "ds_read2_b32 v[194:195], v148 offset0:64 offset1:164\n"
    "v_mfma_f32_16x16x32_bf16 v[36:39], v[224:227], v[16:19], 0\n"
    "v_cvt_pk_bf16_f32 v44, v40, v44\n"
    "ds_read2_b32 v[196:197], v148 offset0:80 offset1:180\n"
    "v_mfma_f32_16x16x32_bf16 v[36:39], v[228:231], v[20:23], v[36:39]\n"
    "v_cvt_pk_bf16_f32 v45, v41, v45\n"
    "v_lshrrev_b32 v150, 4, v57\n"
    "v_mfma_f32_16x16x32_bf16 v[36:39], v[232:235], v[24:27], v[36:39]\n"
    "v_cvt_pk_bf16_f32 v46, v42, v46\n"
    "v_and_b32 v144, 48, v57\n"
    "v_mfma_f32_16x16x32_bf16 v[36:39], v[236:239], v[28:31], v[36:39]\n"
    "v_cvt_pk_bf16_f32 v47, v43, v47\n"
    "v_bitop3_b32 v145, v57, v150, 48 bitop3:0x28\n"
    "v_mfma_f32_16x16x32_bf16 v[40:43], v[240:243], v[0:3], 0\n"
    "ds_write_addtid_b32 v44 offset:5120\n"
    "v_lshl_or_b32 v145, v144, 4, v145\n"
    "v_mfma_f32_16x16x32_bf16 v[40:43], v[244:247], v[4:7], v[40:43]\n"
    "ds_write_addtid_b32 v45 offset:5376\n"
    "v_and_b32 v144, 0xc0, v57\n"
    "v_mfma_f32_16x16x32_bf16 v[40:43], v[248:251], v[8:11], v[40:43]\n"
    "ds_write_addtid_b32 v46 offset:5632\n"
    "v_add3_u32 v145, v145, v144, s54\n"
    "v_mfma_f32_16x16x32_bf16 v[40:43], v[252:255], v[12:15], v[40:43]\n"
    "ds_write_addtid_b32 v47 offset:5888\n"
    "v_add_u32 v151, 65024, v145\n"
    "v_mfma_f32_16x16x32_bf16 v[44:47], v[240:243], v[16:19], 0\n"
    "v_cvt_pk_bf16_f32 v36, v32, v36\n"
    "v_and_b32 v208, s43, v54\n"
    "v_lshrrev_b32 v212, 16, v54\n"
    "ds_read_b128 v[208:211], v208 offset:65520\n"
    "ds_read_b128 v[212:215], v212 offset:65520\n"
    "v_mfma_f32_16x16x32_bf16 v[44:47], v[244:247], v[20:23], v[44:47]\n"
    "v_cvt_pk_bf16_f32 v37, v33, v37\n"
    "v_and_b32 v224, s43, v55\n"
    "v_lshrrev_b32 v228, 16, v55\n"
    "ds_read_b128 v[224:227], v224 offset:65520\n"
    "ds_read_b128 v[228:231], v228 offset:65520\n"
    "v_mfma_f32_16x16x32_bf16 v[44:47], v[248:251], v[24:27], v[44:47]\n"
    "v_cvt_pk_bf16_f32 v38, v34, v38\n"
    "v_mfma_f32_16x16x32_bf16 v[44:47], v[252:255], v[28:31], v[44:47]\n"
    "v_cvt_pk_bf16_f32 v39, v35, v39\n"
    "ds_write_addtid_b32 v36 offset:6144\n"
    "ds_write_addtid_b32 v37 offset:6400\n"
    "ds_write_addtid_b32 v38 offset:6656\n"
    "ds_write_addtid_b32 v39 offset:6912\n"
    "s_nop 2\n"
    "v_cvt_pk_bf16_f32 v44, v40, v44\n"
    "v_cvt_pk_bf16_f32 v45, v41, v45\n"
    "v_cvt_pk_bf16_f32 v46, v42, v46\n"
    "v_cvt_pk_bf16_f32 v47, v43, v47\n"
    "ds_write_addtid_b32 v44 offset:7168\n"
    "ds_write_addtid_b32 v45 offset:7424\n"
    "ds_write_addtid_b32 v46 offset:7680\n"
    "ds_write_addtid_b32 v47 offset:7936\n"
    "s_waitcnt lgkmcnt(15)\n"
    "v_smfmac_f32_16x16x64_bf16 v[112:115], v[60:63], v[128:135], v59\n"
    "s_waitcnt lgkmcnt(15)\n"
    "v_smfmac_f32_16x16x64_bf16 v[116:119], v[60:63], v[160:167], v59\n"
    "s_waitcnt lgkmcnt(10)\n"
    "v_smfmac_f32_16x16x64_bf16 v[120:123], v[60:63], v[208:215], v59\n"
    "s_waitcnt lgkmcnt(8)\n"
    "v_smfmac_f32_16x16x64_bf16 v[124:127], v[60:63], v[224:231], v59\n"
    "v_and_b32 v16, s43, v48\n"
    "v_lshrrev_b32 v20, 16, v48\n"
    "v_and_b32 v24, s43, v49\n"
    "v_lshrrev_b32 v28, 16, v49\n"
    "v_and_b32 v32, s43, v50\n"
    "v_lshrrev_b32 v36, 16, v50\n"
    "v_and_b32 v40, s43, v51\n"
    "v_lshrrev_b32 v44, 16, v51\n"
    "s_setprio 0\n"
    "s_waitcnt lgkmcnt(0)\n"
    "s_mov_b32 exec_lo, 0xeeeeeeee\n"
    "s_mov_b32 exec_hi, 0xeeeeeeee\n"
    "v_mov_b32 v176, 1000\n"
    "v_mov_b32 v177, 1000\n"
    "s_mov_b64 exec, -1\n"
    "v_lshlrev_b32 v0, 2, v176\n"
    "v_lshlrev_b32 v1, 2, v177\n"
    "v_bitop3_b32 v0, v0, v150, 48 bitop3:0x28\n"
    "v_bitop3_b32 v1, v1, v150, 48 bitop3:0x28\n"
    "v_lshl_or_b32 v0, v176, 6, v0\n"
    "v_lshl_or_b32 v1, v177, 6, v1\n"
    "v_lshlrev_b32 v2, 2, v178\n"
    "v_lshlrev_b32 v3, 2, v179\n"
    "v_bitop3_b32 v2, v2, v150, 48 bitop3:0x28\n"
    "v_bitop3_b32 v3, v3, v150, 48 bitop3:0x28\n"
    "v_lshl_or_b32 v2, v178, 6, v2\n"
    "v_lshl_or_b32 v3, v179, 6, v3\n"
    "v_lshlrev_b32 v4, 2, v180\n"
    "v_lshlrev_b32 v5, 2, v181\n"
    "v_bitop3_b32 v4, v4, v150, 48 bitop3:0x28\n"
    "v_bitop3_b32 v5, v5, v150, 48 bitop3:0x28\n"
    "v_lshl_or_b32 v4, v180, 6, v4\n"
    "v_lshl_or_b32 v5, v181, 6, v5\n"
    "v_lshlrev_b32 v6, 2, v182\n"
    "v_lshlrev_b32 v7, 2, v183\n"
    "v_bitop3_b32 v6, v6, v150, 48 bitop3:0x28\n"
    "v_bitop3_b32 v7, v7, v150, 48 bitop3:0x28\n"
    "v_lshl_or_b32 v6, v182, 6, v6\n"
    "v_lshl_or_b32 v7, v183, 6, v7\n"
    "v_lshlrev_b32 v8, 2, v192\n"
    "v_lshlrev_b32 v9, 2, v193\n"
    "v_bitop3_b32 v8, v8, v150, 48 bitop3:0x28\n"
    "v_bitop3_b32 v9, v9, v150, 48 bitop3:0x28\n"
    "v_lshl_or_b32 v8, v192, 6, v8\n"
    "v_lshl_or_b32 v9, v193, 6, v9\n"
    "v_lshlrev_b32 v10, 2, v194\n"
    "v_lshlrev_b32 v11, 2, v195\n"
    "v_bitop3_b32 v10, v10, v150, 48 bitop3:0x28\n"
    "v_bitop3_b32 v11, v11, v150, 48 bitop3:0x28\n"
    "v_lshl_or_b32 v10, v194, 6, v10\n"
    "v_lshl_or_b32 v11, v195, 6, v11\n"
    "v_lshlrev_b32 v12, 2, v196\n"
    "v_lshlrev_b32 v13, 2, v197\n"
    "v_bitop3_b32 v12, v12, v150, 48 bitop3:0x28\n"
    "v_bitop3_b32 v13, v13, v150, 48 bitop3:0x28\n"
    "v_lshl_or_b32 v12, v196, 6, v12\n"
    "v_lshl_or_b32 v13, v197, 6, v13\n"
    "v_bfe_u32 v58, v57, 2, 4\n"
    "v_and_or_b32 v58, v150, 48, v58\n"
    "v_add_u32 v58, v144, v58\n"
    "v_add_u32 v56, 131072, v57\n"
    "v_add_u32 v58, 139392, v58\n"
    "v_lshrrev_b32 v105, 1, v57\n"
    "v_xor_b32 v105, v105, v57\n"
    "v_bfe_u32 v105, v105, 5, 3\n"
    "v_bfe_u32 v106, v57, 5, 2\n"
    "v_and_or_b32 v105, v105, 4, v106\n"
    "v_mov_b32 v106, 0x3f80\n"
    "v_cmp_eq_u32_e64 vcc, 0, v105\n"
    "v_cmp_eq_u32_e64 s[14:15], 1, v105\n"
    "v_mov_b32 v104, 0xbdd2d3e8\n"
    "v_lshrrev_b32 v110, 4, v57\n"
    "v_cndmask_b32_e64 v96, 0, v106, vcc\n"
    "v_cndmask_b32_e64 v97, 0, v106, s[14:15]\n"
    "v_cmp_eq_u32_e64 vcc, 2, v105\n"
    "v_cmp_eq_u32_e64 s[14:15], 3, v105\n"
    "v_lshlrev_b32 v108, 12, v57\n"
    "v_bfe_u32 v105, v57, 4, 1\n"
    "v_cndmask_b32_e64 v98, 0, v106, vcc\n"
    "v_cndmask_b32_e64 v99, 0, v106, s[14:15]\n"
    "v_mul_u32_u24 v105, 0x5555, v105\n"
    "v_or_b32 v106, 0xaaaa, v105\n"
    "v_and_b32 v110, 32, v110\n"
    "v_lshl_add_u32 v111, s10, 2, v110\n"
    "v_add_u32 v111, 163264, v111\n"
    "v_add_u32 v110, s53, v110\n"
    "ds_read_b32 v111, v111\n"
    "v_lshlrev_b32_e64 v100, 16, s24\n"
    "s_and_b32 s24, s24, 0xffff0000\n"
    "v_lshlrev_b32_e64 v102, 16, s25\n"
    "s_and_b32 s25, s25, 0xffff0000\n"
    "v_mov_b32 v84, s20\n"
    "v_mov_b32 v85, s20\n"
    "v_mov_b32 v86, s21\n"
    "v_mov_b32 v87, s21\n"
    "v_mov_b32 v101, s24\n"
    "v_mov_b32 v103, s25\n"
    "s_mov_b32 exec_lo, 0xffff0000\n"
    "s_mov_b32 exec_hi, 0xffff0000\n"
    "v_lshlrev_b32_e64 v100, 16, s100\n"
    "s_and_b32 s100, s100, 0xffff0000\n"
    "v_lshlrev_b32_e64 v102, 16, s101\n"
    "s_and_b32 s101, s101, 0xffff0000\n"
    "v_mov_b32 v84, s22\n"
    "v_mov_b32 v85, s22\n"
    "v_mov_b32 v86, s23\n"
    "v_mov_b32 v87, s23\n"
    "v_mov_b32 v101, s100\n"
    "v_mov_b32 v103, s101\n"
    "s_mov_b64 exec, -1\n"
    "s_waitcnt vmcnt(0)\n"
    "s_mov_b32 s66, 65536\n"
    "L_p2Aa_%=:\n"
    "v_cmp_ne_u64_e64 s[14:15], v[90:91], s[12:13]\n"
    "s_nop 3\n"
    "s_cmp_eq_u64 s[14:15], 0\n"
    "s_cbranch_scc1 L_p2Ab_%=\n"
    "s_sleep 2\n"
    "buffer_load_dwordx2 v[90:91], v88, s[48:51], s57 offen sc1\n"
    "s_waitcnt vmcnt(0)\n"
    "s_sub_u32 s66, s66, 1\n"
    "s_cmp_lg_u32 s66, 0\n"
    "s_cbranch_scc1 L_p2Aa_%=\n"
    "L_p2Ab_%=:\n"
    "s_waitcnt lgkmcnt(0)\n"
    "s_barrier\n"
    "s_setprio 3\n"
    "ds_read_b128 v[16:19], v16\n"
    "ds_read_b128 v[20:23], v20\n"
    "ds_read_b128 v[24:27], v24\n"
    "ds_read_b128 v[28:31], v28\n"
    "ds_read_b128 v[32:35], v32\n"
    "ds_read_b128 v[36:39], v36\n"
    "ds_read_b128 v[40:43], v40\n"
    "ds_read_b128 v[44:47], v44\n"
    "buffer_load_dwordx4 v[64:67], v57, s[48:51], s59 offen sc1\n"
    "buffer_load_dwordx4 v[72:75], v57, s[48:51], s59 offen offset:1024 sc1\n"
    "buffer_load_dwordx4 v[68:71], v57, s[48:51], s60 offen sc1\n"
    "buffer_load_dwordx4 v[76:79], v57, s[48:51], s60 offen offset:1024 sc1\n"
    "buffer_load_dwordx4 v[80:83], v57, s[48:51], s61 offen sc1\n"
    "buffer_load_dwordx4 v[88:91], v57, s[48:51], s61 offen offset:1024 sc1\n"
    "ds_write_b128 v151, v[152:155] offset:1024\n"
    "s_add_u32 m0, s35, 131072\n"
    "s_nop 0\n"
    "buffer_load_dwordx4 v57, s[48:51], s34 offen sc1 lds\n"
    "buffer_load_dwordx4 v57, s[48:51], s34 offen offset:1024 sc1 lds\n"
    "ds_write_b128 v151, v[168:171] offset:2048\n"
    "ds_write_b128 v151, v[184:187] offset:3072\n"
    "ds_write_b128 v151, v[200:203] offset:4096\n"
    "s_waitcnt lgkmcnt(4)\n"
    "s_barrier\n"
    "ds_write_b128 v151, v[216:219] offset:5120\n"
    "v_smfmac_f32_16x16x64_bf16 v[112:115], v[60:63], v[16:23], v59\n"
    "v_smfmac_f32_16x16x64_bf16 v[116:119], v[60:63], v[24:31], v59\n"
    "v_smfmac_f32_16x16x64_bf16 v[120:123], v[60:63], v[32:39], v59\n"
    "v_smfmac_f32_16x16x64_bf16 v[124:127], v[60:63], v[40:47], v59\n"
    "s_waitcnt vmcnt(2)\n"
    "s_mov_b32 s66, 65536\n"
    "L_iaA_%=:\n"
    "v_max3_u32 v14, v64, v72, v68\n"
    "v_max3_u32 v15, v76, v80, v88\n"
    "v_max_u32 v14, v14, v15\n"
    "v_cmp_eq_u32_e64 vcc, -1, v14\n"
    "s_nop 3\n"
    "s_cbranch_vccz L_ibA_%=\n"
    "s_sleep 4\n"
    "buffer_load_dwordx4 v[64:67], v57, s[48:51], s59 offen sc1\n"
    "buffer_load_dwordx4 v[72:75], v57, s[48:51], s59 offen offset:1024 sc1\n"
    "buffer_load_dwordx4 v[68:71], v57, s[48:51], s60 offen sc1\n"
    "buffer_load_dwordx4 v[76:79], v57, s[48:51], s60 offen offset:1024 sc1\n"
    "buffer_load_dwordx4 v[80:83], v57, s[48:51], s61 offen sc1\n"
    "buffer_load_dwordx4 v[88:91], v57, s[48:51], s61 offen offset:1024 sc1\n"
    "s_waitcnt vmcnt(0)\n"
    "s_sub_u32 s66, s66, 1\n"
    "s_cmp_lg_u32 s66, 0\n"
    "s_cbranch_scc1 L_iaA_%=\n"
    "L_ibA_%=:\n"
    "s_mov_b32 s40, 0xc0135761\n"
    "v_smfmac_f32_16x16x64_bf16 v[112:115], v[96:99], v[64:71], v105\n"
    "v_smfmac_f32_16x16x64_bf16 v[116:119], v[96:99], v[64:71], v106\n"
    "v_smfmac_f32_16x16x64_bf16 v[120:123], v[96:99], v[72:79], v105\n"
    "v_smfmac_f32_16x16x64_bf16 v[124:127], v[96:99], v[72:79], v106\n"
    "v_smfmac_f32_16x16x64_bf16 v[112:115], v[96:99], v[80:87], v105\n"
    "v_smfmac_f32_16x16x64_bf16 v[116:119], v[96:99], v[80:87], v106\n"
    "v_smfmac_f32_16x16x64_bf16 v[120:123], v[96:99], v[84:91], v105\n"
    "v_smfmac_f32_16x16x64_bf16 v[124:127], v[96:99], v[84:91], v106\n"
    "ds_write_b128 v151, v[232:235] offset:6144\n"
    "s_nop 3\n"
    "v_pk_mul_f32 v[64:65], v[112:113], v[112:113]\n"
    "v_pk_mul_f32 v[66:67], v[114:115], v[114:115]\n"
    "v_pk_mul_f32 v[68:69], v[116:117], v[116:117]\n"
    "v_pk_mul_f32 v[70:71], v[118:119], v[118:119]\n"
    "v_pk_mul_f32 v[72:73], v[120:121], v[120:121]\n"
    "v_pk_mul_f32 v[74:75], v[122:123], v[122:123]\n"
    "v_pk_mul_f32 v[76:77], v[124:125], v[124:125]\n"
    "v_pk_mul_f32 v[78:79], v[126:127], v[126:127]\n"
    "v_pk_fma_f32 v[64:65], v[64:65], v[104:105], s[40:41] op_sel_hi:[1,0,0]\n"
    "v_pk_fma_f32 v[66:67], v[66:67], v[104:105], s[40:41] op_sel_hi:[1,0,0]\n"
    "ds_write_b128 v151, v[248:251] offset:7168\n"
    "v_pk_fma_f32 v[68:69], v[68:69], v[104:105], s[40:41] op_sel_hi:[1,0,0]\n"
    "v_pk_fma_f32 v[70:71], v[70:71], v[104:105], s[40:41] op_sel_hi:[1,0,0]\n"
    "v_pk_fma_f32 v[72:73], v[72:73], v[104:105], s[40:41] op_sel_hi:[1,0,0]\n"
    "v_pk_fma_f32 v[74:75], v[74:75], v[104:105], s[40:41] op_sel_hi:[1,0,0]\n"
    "v_pk_fma_f32 v[76:77], v[76:77], v[104:105], s[40:41] op_sel_hi:[1,0,0]\n"
    "v_pk_fma_f32 v[78:79], v[78:79], v[104:105], s[40:41] op_sel_hi:[1,0,0]\n"
    "v_pk_mul_f32 v[64:65], v[64:65], v[112:113]\n"
    "v_pk_mul_f32 v[66:67], v[66:67], v[114:115]\n"
    "v_pk_mul_f32 v[68:69], v[68:69], v[116:117]\n"
    "v_pk_mul_f32 v[70:71], v[70:71], v[118:119]\n"
    "ds_write_b128 v151, v[136:139]\n"
    "v_pk_mul_f32 v[72:73], v[72:73], v[120:121]\n"
    "v_pk_mul_f32 v[74:75], v[74:75], v[122:123]\n"
    "v_pk_mul_f32 v[76:77], v[76:77], v[124:125]\n"
    "v_pk_mul_f32 v[78:79], v[78:79], v[126:127]\n"
    "v_exp_f32 v64, v64\n"
    "v_pk_mul_f32 v[112:113], v[112:113], v[100:101]\n"
    "v_exp_f32 v65, v65\n"
    "v_exp_f32 v66, v66\n"
    "v_pk_mul_f32 v[114:115], v[114:115], v[102:103]\n"
    "v_exp_f32 v67, v67\n"
    "ds_write_b128 v145, v[140:143]\n"
    "v_exp_f32 v68, v68\n"
    "v_pk_mul_f32 v[116:117], v[116:117], v[100:101]\n"
    "v_exp_f32 v69, v69\n"
    "v_exp_f32 v70, v70\n"
    "v_pk_mul_f32 v[118:119], v[118:119], v[102:103]\n"
    "v_exp_f32 v71, v71\n"
    "v_exp_f32 v72, v72\n"
    "v_pk_mul_f32 v[120:121], v[120:121], v[100:101]\n"
    "v_exp_f32 v73, v73\n"
    "v_exp_f32 v74, v74\n"
    "ds_write_b128 v145, v[156:159] offset:1024\n"
    "v_pk_mul_f32 v[122:123], v[122:123], v[102:103]\n"
    "v_exp_f32 v75, v75\n"
    "v_exp_f32 v76, v76\n"
    "v_pk_mul_f32 v[124:125], v[124:125], v[100:101]\n"
    "v_exp_f32 v77, v77\n"
    "v_exp_f32 v78, v78\n"
    "v_pk_mul_f32 v[126:127], v[126:127], v[102:103]\n"
    "v_exp_f32 v79, v79\n"
    "v_pk_add_f32 v[64:65], v[64:65], 1.0 op_sel_hi:[1,0]\n"
    "v_pk_add_f32 v[66:67], v[66:67], 1.0 op_sel_hi:[1,0]\n"
    "ds_write_b128 v145, v[172:175] offset:2048\n"
    "v_pk_add_f32 v[68:69], v[68:69], 1.0 op_sel_hi:[1,0]\n"
    "v_pk_add_f32 v[70:71], v[70:71], 1.0 op_sel_hi:[1,0]\n"
    "v_pk_add_f32 v[72:73], v[72:73], 1.0 op_sel_hi:[1,0]\n"
    "v_pk_add_f32 v[74:75], v[74:75], 1.0 op_sel_hi:[1,0]\n"
    "v_pk_add_f32 v[76:77], v[76:77], 1.0 op_sel_hi:[1,0]\n"
    "v_pk_add_f32 v[78:79], v[78:79], 1.0 op_sel_hi:[1,0]\n"
    "v_rcp_f32 v64, v64\n"
    "v_rcp_f32 v65, v65\n"
    "v_rcp_f32 v66, v66\n"
    "v_rcp_f32 v67, v67\n"
    "ds_write_b128 v145, v[188:191] offset:3072\n"
    "v_rcp_f32 v68, v68\n"
    "v_rcp_f32 v69, v69\n"
    "v_rcp_f32 v70, v70\n"
    "v_rcp_f32 v71, v71\n"
    "v_rcp_f32 v72, v72\n"
    "v_rcp_f32 v73, v73\n"
    "v_rcp_f32 v74, v74\n"
    "v_rcp_f32 v75, v75\n"
    "v_rcp_f32 v76, v76\n"
    "v_rcp_f32 v77, v77\n"
    "ds_write_b128 v145, v[204:207] offset:4096\n"
    "v_rcp_f32 v78, v78\n"
    "v_rcp_f32 v79, v79\n"
    "v_mov_b32 v81, s12\n"
    "v_mov_b32 v83, s13\n"
    "v_pk_mul_f32 v[64:65], v[64:65], v[112:113]\n"
    "v_pk_mul_f32 v[68:69], v[68:69], v[116:117]\n"
    "v_pk_mul_f32 v[72:73], v[72:73], v[120:121]\n"
    "v_pk_mul_f32 v[76:77], v[76:77], v[124:125]\n"
    "v_pk_fma_f32 v[64:65], v[66:67], v[114:115], v[64:65]\n"
    "v_pk_fma_f32 v[68:69], v[70:71], v[118:119], v[68:69]\n"
    "ds_write_b128 v145, v[220:223] offset:5120\n"
    "v_pk_fma_f32 v[72:73], v[74:75], v[122:123], v[72:73]\n"
    "v_pk_fma_f32 v[76:77], v[78:79], v[126:127], v[76:77]\n"
    "v_add_f32 v80, v64, v65\n"
    "v_add_f32 v82, v68, v69\n"
    "v_add_f32 v84, v72, v73\n"
    "v_add_f32 v85, v76, v77\n"
    "s_nop 0\n"
    "v_permlane16_swap_b32 v80, v84\n"
    "v_permlane16_swap_b32 v82, v85\n"
    "v_add_f32 v80, v80, v84\n"
    "v_add_f32 v82, v82, v85\n"
    "ds_write_b128 v145, v[236:239] offset:6144\n"
    "buffer_store_dwordx4 v[80:83], v57, s[48:51], s62 offen sc1\n"
    "ds_write_b128 v145, v[252:255] offset:7168\n"
    "s_waitcnt vmcnt(1)\n"
    "s_waitcnt lgkmcnt(0)\n"
    "s_barrier\n"
    "s_setprio 3\n"
    "ds_read_b128 v[128:131], v0 offset:65024\n"
    "ds_read_b128 v[132:135], v1 offset:65024\n"
    "ds_read_b128 v[136:139], v0\n"
    "ds_read_b128 v[140:143], v1\n"
    "ds_read_b128 v[144:147], v2 offset:65024\n"
    "ds_read_b128 v[148:151], v3 offset:65024\n"
    "ds_read_b128 v[152:155], v2\n"
    "ds_read_b128 v[156:159], v3\n"
    "s_setprio 3\n"
    "s_setprio 3\n"
    "s_setprio 3\n"
    "s_waitcnt lgkmcnt(7)\n"
    "v_mfma_f32_16x16x32_bf16 v[16:19], v[128:131], v[128:131], 0\n"
    "ds_read_b32 v40, v58 offset:1536\n"
    "ds_read_b128 v[160:163], v4 offset:65024\n"
    "s_waitcnt lgkmcnt(7)\n"
    "v_mfma_f32_16x16x32_bf16 v[16:19], v[136:139], v[136:139], v[16:19]\n"
    "v_mfma_f32_16x16x32_bf16 v[20:23], v[132:135], v[132:135], 0\n"
    "s_waitcnt lgkmcnt(6)\n"
    "v_mfma_f32_16x16x32_bf16 v[20:23], v[140:143], v[140:143], v[20:23]\n"
    "ds_read_b128 v[164:167], v5 offset:65024\n"
    "s_waitcnt lgkmcnt(6)\n"
    "v_mfma_f32_16x16x32_bf16 v[24:27], v[128:131], v[144:147], 0\n"
    "ds_read_b32 v44, v58\n"
    "s_waitcnt lgkmcnt(5)\n"
    "v_mfma_f32_16x16x32_bf16 v[24:27], v[136:139], v[152:155], v[24:27]\n"
    "v_mfma_f32_16x16x32_bf16 v[28:31], v[132:135], v[148:151], 0\n"
    "ds_read_b128 v[168:171], v4\n"
    "s_waitcnt lgkmcnt(5)\n"
    "v_mfma_f32_16x16x32_bf16 v[28:31], v[140:143], v[156:159], v[28:31]\n"
    "v_mfma_f32_16x16x32_bf16 v[32:35], v[144:147], v[144:147], 0\n"
    "ds_read_b128 v[48:51], v56 offset:10368\n"
    "s_waitcnt lgkmcnt(5)\n"
    "v_mul_f32 v52, v16, v40\n"
    "v_mfma_f32_16x16x32_bf16 v[32:35], v[152:155], v[152:155], v[32:35]\n"
    "ds_read_b128 v[172:175], v5\n"
    "v_mfma_f32_16x16x32_bf16 v[36:39], v[148:151], v[148:151], 0\n"
    "v_mul_f32 v54, v20, v40\n"
    "v_mfma_f32_16x16x32_bf16 v[36:39], v[156:159], v[156:159], v[36:39]\n"
    "s_waitcnt lgkmcnt(5)\n"
    "v_mfma_f32_16x16x32_bf16 v[16:19], v[128:131], v[160:163], 0\n"
    "ds_read_b32 v40, v58 offset:256\n"
    "s_waitcnt lgkmcnt(4)\n"
    "v_fmac_f32 v52, v24, v44\n"
    "ds_read_b128 v[176:179], v6 offset:65024\n"
    "s_waitcnt lgkmcnt(4)\n"
    "v_mfma_f32_16x16x32_bf16 v[16:19], v[136:139], v[168:171], v[16:19]\n"
    "v_mfma_f32_16x16x32_bf16 v[20:23], v[132:135], v[164:167], 0\n"
    "v_fmac_f32 v54, v28, v44\n"
    "s_waitcnt lgkmcnt(2)\n"
    "v_mfma_f32_16x16x32_bf16 v[20:23], v[140:143], v[172:175], v[20:23]\n"
    "ds_read_b128 v[180:183], v7 offset:65024\n"
    "v_mfma_f32_16x16x32_bf16 v[24:27], v[144:147], v[160:163], 0\n"
    "ds_read_b128 v[44:47], v56 offset:11392\n"
    "v_fmac_f32 v52, v32, v48\n"
    "v_mul_f32 v53, v33, v49\n"
    "v_mfma_f32_16x16x32_bf16 v[24:27], v[152:155], v[168:171], v[24:27]\n"
    "v_fmac_f32 v52, v34, v50\n"
    "v_fmac_f32 v53, v35, v51\n"
    "v_mfma_f32_16x16x32_bf16 v[28:31], v[148:151], v[164:167], 0\n"
    "v_fmac_f32 v54, v36, v48\n"
    "v_mul_f32 v55, v37, v49\n"
    "ds_read_b128 v[184:187], v6\n"
    "v_mfma_f32_16x16x32_bf16 v[28:31], v[156:159], v[172:175], v[28:31]\n"
    "v_fmac_f32 v54, v38, v50\n"
    "v_fmac_f32 v55, v39, v51\n"
    "v_mfma_f32_16x16x32_bf16 v[32:35], v[160:163], v[160:163], 0\n"
    "ds_read_b128 v[48:51], v56 offset:12416\n"
    "s_waitcnt lgkmcnt(5)\n"
    "v_fmac_f32 v52, v16, v40\n"
    "v_mfma_f32_16x16x32_bf16 v[32:35], v[168:171], v[168:171], v[32:35]\n"
    "ds_read_b128 v[188:191], v7\n"
    "v_mfma_f32_16x16x32_bf16 v[36:39], v[164:167], v[164:167], 0\n"
    "v_fmac_f32 v54, v20, v40\n"
    "v_mfma_f32_16x16x32_bf16 v[36:39], v[172:175], v[172:175], v[36:39]\n"
    "s_waitcnt lgkmcnt(5)\n"
    "v_mfma_f32_16x16x32_bf16 v[16:19], v[128:131], v[176:179], 0\n"
    "ds_read_b32 v40, v58 offset:512\n"
    "s_waitcnt lgkmcnt(4)\n"
    "v_fmac_f32 v52, v24, v44\n"
    "v_fmac_f32 v53, v25, v45\n"
    "s_waitcnt lgkmcnt(3)\n"
    "v_mfma_f32_16x16x32_bf16 v[16:19], v[136:139], v[184:187], v[16:19]\n"
    "v_fmac_f32 v52, v26, v46\n"
    "v_fmac_f32 v53, v27, v47\n"
    "ds_read_b128 v[192:195], v8 offset:65024\n"
    "v_mfma_f32_16x16x32_bf16 v[20:23], v[132:135], v[180:183], 0\n"
    "v_fmac_f32 v54, v28, v44\n"
    "v_fmac_f32 v55, v29, v45\n"
    "s_waitcnt lgkmcnt(2)\n"
    "v_mfma_f32_16x16x32_bf16 v[20:23], v[140:143], v[188:191], v[20:23]\n"
    "v_fmac_f32 v54, v30, v46\n"
    "v_fmac_f32 v55, v31, v47\n"
    "ds_read_b128 v[196:199], v9 offset:65024\n"
    "v_mfma_f32_16x16x32_bf16 v[24:27], v[144:147], v[176:179], 0\n"
    "ds_read_b128 v[44:47], v56 offset:13440\n"
    "v_fmac_f32 v52, v32, v48\n"
    "v_fmac_f32 v53, v33, v49\n"
    "v_mfma_f32_16x16x32_bf16 v[24:27], v[152:155], v[184:187], v[24:27]\n"
    "v_fmac_f32 v52, v34, v50\n"
    "v_fmac_f32 v53, v35, v51\n"
    "v_mfma_f32_16x16x32_bf16 v[28:31], v[148:151], v[180:183], 0\n"
    "v_fmac_f32 v54, v36, v48\n"
    "v_fmac_f32 v55, v37, v49\n"
    "ds_read_b128 v[200:203], v8\n"
    "v_mfma_f32_16x16x32_bf16 v[28:31], v[156:159], v[188:191], v[28:31]\n"
    "v_fmac_f32 v54, v38, v50\n"
    "v_fmac_f32 v55, v39, v51\n"
    "v_mfma_f32_16x16x32_bf16 v[32:35], v[160:163], v[176:179], 0\n"
    "ds_read_b128 v[48:51], v56 offset:14464\n"
    "s_waitcnt lgkmcnt(5)\n"
    "v_fmac_f32 v52, v16, v40\n"
    "v_mfma_f32_16x16x32_bf16 v[32:35], v[168:171], v[184:187], v[32:35]\n"
    "ds_read_b128 v[204:207], v9\n"
    "v_mfma_f32_16x16x32_bf16 v[36:39], v[164:167], v[180:183], 0\n"
    "v_fmac_f32 v54, v20, v40\n"
    "v_mfma_f32_16x16x32_bf16 v[36:39], v[172:175], v[188:191], v[36:39]\n"
    "s_setprio 2\n"
    "v_mfma_f32_16x16x32_bf16 v[16:19], v[176:179], v[176:179], 0\n"
    "ds_read_b128 v[40:43], v56 offset:15488\n"
    "s_waitcnt lgkmcnt(4)\n"
    "v_fmac_f32 v52, v24, v44\n"
    "v_fmac_f32 v53, v25, v45\n"
    "v_mfma_f32_16x16x32_bf16 v[16:19], v[184:187], v[184:187], v[16:19]\n"
    "v_fmac_f32 v52, v26, v46\n"
    "v_fmac_f32 v53, v27, v47\n"
    "v_mfma_f32_16x16x32_bf16 v[20:23], v[180:183], v[180:183], 0\n"
    "v_fmac_f32 v54, v28, v44\n"
    "v_fmac_f32 v55, v29, v45\n"
    "v_mfma_f32_16x16x32_bf16 v[20:23], v[188:191], v[188:191], v[20:23]\n"
    "v_fmac_f32 v54, v30, v46\n"
    "v_fmac_f32 v55, v31, v47\n"
    "v_mfma_f32_16x16x32_bf16 v[24:27], v[128:131], v[192:195], 0\n"
    "ds_read_b32 v44, v58 offset:768\n"
    "s_waitcnt lgkmcnt(3)\n"
    "v_fmac_f32 v52, v32, v48\n"
    "v_fmac_f32 v53, v33, v49\n"
    "v_mfma_f32_16x16x32_bf16 v[24:27], v[136:139], v[200:203], v[24:27]\n"
    "v_fmac_f32 v52, v34, v50\n"
    "v_fmac_f32 v53, v35, v51\n"
    "v_mfma_f32_16x16x32_bf16 v[28:31], v[132:135], v[196:199], 0\n"
    "v_fmac_f32 v54, v36, v48\n"
    "v_fmac_f32 v55, v37, v49\n"
    "s_waitcnt lgkmcnt(2)\n"
    "v_mfma_f32_16x16x32_bf16 v[28:31], v[140:143], v[204:207], v[28:31]\n"
    "v_fmac_f32 v54, v38, v50\n"
    "v_fmac_f32 v55, v39, v51\n"
    "v_mfma_f32_16x16x32_bf16 v[32:35], v[144:147], v[192:195], 0\n"
    "ds_read_b128 v[48:51], v56 offset:16640\n"
    "s_waitcnt lgkmcnt(2)\n"
    "v_fmac_f32 v52, v16, v40\n"
    "v_fmac_f32 v53, v17, v41\n"
    "v_mfma_f32_16x16x32_bf16 v[32:35], v[152:155], v[200:203], v[32:35]\n"
    "v_fmac_f32 v52, v18, v42\n"
    "v_fmac_f32 v53, v19, v43\n"
    "ds_read_b128 v[208:211], v10 offset:65024\n"
    "v_mfma_f32_16x16x32_bf16 v[36:39], v[148:151], v[196:199], 0\n"
    "v_fmac_f32 v54, v20, v40\n"
    "v_fmac_f32 v55, v21, v41\n"
    "v_mfma_f32_16x16x32_bf16 v[36:39], v[156:159], v[204:207], v[36:39]\n"
    "v_fmac_f32 v54, v22, v42\n"
    "v_fmac_f32 v55, v23, v43\n"
    "ds_read_b128 v[212:215], v11 offset:65024\n"
    "v_mfma_f32_16x16x32_bf16 v[16:19], v[160:163], v[192:195], 0\n"
    "ds_read_b128 v[40:43], v56 offset:17664\n"
    "s_waitcnt lgkmcnt(4)\n"
    "v_fmac_f32 v52, v24, v44\n"
    "v_mfma_f32_16x16x32_bf16 v[16:19], v[168:171], v[200:203], v[16:19]\n"
    "ds_read_b128 v[216:219], v10\n"
    "v_mfma_f32_16x16x32_bf16 v[20:23], v[164:167], v[196:199], 0\n"
    "v_fmac_f32 v54, v28, v44\n"
    "v_mfma_f32_16x16x32_bf16 v[20:23], v[172:175], v[204:207], v[20:23]\n"
    "ds_read_b128 v[220:223], v11\n"
    "v_mfma_f32_16x16x32_bf16 v[24:27], v[176:179], v[192:195], 0\n"
    "ds_read_b128 v[44:47], v56 offset:18688\n"
    "s_waitcnt lgkmcnt(6)\n"
    "v_fmac_f32 v52, v32, v48\n"
    "v_fmac_f32 v53, v33, v49\n"
    "v_mfma_f32_16x16x32_bf16 v[24:27], v[184:187], v[200:203], v[24:27]\n"
    "v_fmac_f32 v52, v34, v50\n"
    "v_fmac_f32 v53, v35, v51\n"
    "v_mfma_f32_16x16x32_bf16 v[28:31], v[180:183], v[196:199], 0\n"
    "v_fmac_f32 v54, v36, v48\n"
    "v_fmac_f32 v55, v37, v49\n"
    "v_mfma_f32_16x16x32_bf16 v[28:31], v[188:191], v[204:207], v[28:31]\n"
    "v_fmac_f32 v54, v38, v50\n"
    "v_fmac_f32 v55, v39, v51\n"
    "v_mfma_f32_16x16x32_bf16 v[32:35], v[192:195], v[192:195], 0\n"
    "ds_read_b128 v[48:51], v56 offset:19712\n"
    "s_waitcnt lgkmcnt(4)\n"
    "v_fmac_f32 v52, v16, v40\n"
    "v_fmac_f32 v53, v17, v41\n"
    "v_mfma_f32_16x16x32_bf16 v[32:35], v[200:203], v[200:203], v[32:35]\n"
    "v_fmac_f32 v52, v18, v42\n"
    "v_fmac_f32 v53, v19, v43\n"
    "v_mfma_f32_16x16x32_bf16 v[36:39], v[196:199], v[196:199], 0\n"
    "v_fmac_f32 v54, v20, v40\n"
    "v_fmac_f32 v55, v21, v41\n"
    "v_mfma_f32_16x16x32_bf16 v[36:39], v[204:207], v[204:207], v[36:39]\n"
    "v_fmac_f32 v54, v22, v42\n"
    "v_fmac_f32 v55, v23, v43\n"
    "v_mfma_f32_16x16x32_bf16 v[16:19], v[144:147], v[208:211], 0\n"
    "ds_read_b128 v[40:43], v56 offset:20736\n"
    "s_waitcnt lgkmcnt(2)\n"
    "v_fmac_f32 v52, v24, v44\n"
    "v_fmac_f32 v53, v25, v45\n"
    "v_mfma_f32_16x16x32_bf16 v[16:19], v[152:155], v[216:219], v[16:19]\n"
    "v_fmac_f32 v52, v26, v46\n"
    "v_fmac_f32 v53, v27, v47\n"
    "v_mfma_f32_16x16x32_bf16 v[20:23], v[148:151], v[212:215], 0\n"
    "v_fmac_f32 v54, v28, v44\n"
    "v_fmac_f32 v55, v29, v45\n"
    "v_mfma_f32_16x16x32_bf16 v[20:23], v[156:159], v[220:223], v[20:23]\n"
    "v_fmac_f32 v54, v30, v46\n"
    "v_fmac_f32 v55, v31, v47\n"
    "s_setprio 1\n"
    "v_mfma_f32_16x16x32_bf16 v[24:27], v[160:163], v[208:211], 0\n"
    "ds_read_b128 v[44:47], v56 offset:21760\n"
    "s_waitcnt lgkmcnt(2)\n"
    "v_fmac_f32 v52, v32, v48\n"
    "v_fmac_f32 v53, v33, v49\n"
    "v_mfma_f32_16x16x32_bf16 v[24:27], v[168:171], v[216:219], v[24:27]\n"
    "v_fmac_f32 v52, v34, v50\n"
    "v_fmac_f32 v53, v35, v51\n"
    "v_mfma_f32_16x16x32_bf16 v[28:31], v[164:167], v[212:215], 0\n"
    "v_fmac_f32 v54, v36, v48\n"
    "v_fmac_f32 v55, v37, v49\n"
    "v_mfma_f32_16x16x32_bf16 v[28:31], v[172:175], v[220:223], v[28:31]\n"
    "v_fmac_f32 v54, v38, v50\n"
    "v_fmac_f32 v55, v39, v51\n"
    "v_mfma_f32_16x16x32_bf16 v[32:35], v[176:179], v[208:211], 0\n"
    "ds_read_b128 v[48:51], v56 offset:22784\n"
    "s_waitcnt lgkmcnt(2)\n"
    "v_fmac_f32 v52, v16, v40\n"
    "v_fmac_f32 v53, v17, v41\n"
    "v_mfma_f32_16x16x32_bf16 v[32:35], v[184:187], v[216:219], v[32:35]\n"
    "v_fmac_f32 v52, v18, v42\n"
    "v_fmac_f32 v53, v19, v43\n"
    "ds_read_b128 v[224:227], v12 offset:65024\n"
    "v_mfma_f32_16x16x32_bf16 v[36:39], v[180:183], v[212:215], 0\n"
    "v_fmac_f32 v54, v20, v40\n"
    "v_fmac_f32 v55, v21, v41\n"
    "v_mfma_f32_16x16x32_bf16 v[36:39], v[188:191], v[220:223], v[36:39]\n"
    "v_fmac_f32 v54, v22, v42\n"
    "v_fmac_f32 v55, v23, v43\n"
    "ds_read_b128 v[228:231], v13 offset:65024\n"
    "v_mfma_f32_16x16x32_bf16 v[16:19], v[192:195], v[208:211], 0\n"
    "ds_read_b128 v[40:43], v56 offset:23808\n"
    "s_waitcnt lgkmcnt(4)\n"
    "v_fmac_f32 v52, v24, v44\n"
    "v_fmac_f32 v53, v25, v45\n"
    "v_mfma_f32_16x16x32_bf16 v[16:19], v[200:203], v[216:219], v[16:19]\n"
    "v_fmac_f32 v52, v26, v46\n"
    "v_fmac_f32 v53, v27, v47\n"
    "ds_read_b128 v[232:235], v12\n"
    "v_mfma_f32_16x16x32_bf16 v[20:23], v[196:199], v[212:215], 0\n"
    "v_fmac_f32 v54, v28, v44\n"
    "v_fmac_f32 v55, v29, v45\n"
    "v_mfma_f32_16x16x32_bf16 v[20:23], v[204:207], v[220:223], v[20:23]\n"
    "v_fmac_f32 v54, v30, v46\n"
    "v_fmac_f32 v55, v31, v47\n"
    "ds_read_b128 v[236:239], v13\n"
    "v_mfma_f32_16x16x32_bf16 v[24:27], v[208:211], v[208:211], 0\n"
    "ds_read_b128 v[44:47], v56\n"
    "s_waitcnt lgkmcnt(6)\n"
    "v_fmac_f32 v52, v32, v48\n"
    "v_fmac_f32 v53, v33, v49\n"
    "v_mfma_f32_16x16x32_bf16 v[24:27], v[216:219], v[216:219], v[24:27]\n"
    "v_fmac_f32 v52, v34, v50\n"
    "v_fmac_f32 v53, v35, v51\n"
    "v_mfma_f32_16x16x32_bf16 v[28:31], v[212:215], v[212:215], 0\n"
    "v_fmac_f32 v54, v36, v48\n"
    "v_fmac_f32 v55, v37, v49\n"
    "v_mfma_f32_16x16x32_bf16 v[28:31], v[220:223], v[220:223], v[28:31]\n"
    "v_fmac_f32 v54, v38, v50\n"
    "v_fmac_f32 v55, v39, v51\n"
    "buffer_load_dwordx4 v[92:95], v108, s[48:51], s63 offen sc1\n"
    "v_add_f32 v111, s98, v111\n"
    "s_waitcnt lgkmcnt(5)\n"
    "v_mfma_f32_16x16x32_bf16 v[32:35], v[144:147], v[224:227], 0\n"
    "ds_read_b128 v[48:51], v56 offset:1024\n"
    "s_waitcnt lgkmcnt(4)\n"
    "v_fmac_f32 v52, v16, v40\n"
    "v_fmac_f32 v53, v17, v41\n"
    "s_waitcnt lgkmcnt(3)\n"
    "v_mfma_f32_16x16x32_bf16 v[32:35], v[152:155], v[232:235], v[32:35]\n"
    "v_fmac_f32 v52, v18, v42\n"
    "v_fmac_f32 v53, v19, v43\n"
    "v_mfma_f32_16x16x32_bf16 v[36:39], v[148:151], v[228:231], 0\n"
    "v_fmac_f32 v54, v20, v40\n"
    "v_fmac_f32 v55, v21, v41\n"
    "s_waitcnt lgkmcnt(2)\n"
    "v_mfma_f32_16x16x32_bf16 v[36:39], v[156:159], v[236:239], v[36:39]\n"
    "v_fmac_f32 v54, v22, v42\n"
    "v_fmac_f32 v55, v23, v43\n"
    "v_mfma_f32_16x16x32_bf16 v[16:19], v[160:163], v[224:227], 0\n"
    "ds_read_b128 v[40:43], v56 offset:2048\n"
    "s_waitcnt lgkmcnt(2)\n"
    "v_fmac_f32 v52, v24, v44\n"
    "v_fmac_f32 v53, v25, v45\n"
    "v_mfma_f32_16x16x32_bf16 v[16:19], v[168:171], v[232:235], v[16:19]\n"
    "v_fmac_f32 v52, v26, v46\n"
    "v_fmac_f32 v53, v27, v47\n"
    "v_mfma_f32_16x16x32_bf16 v[20:23], v[164:167], v[228:231], 0\n"
    "v_fmac_f32 v54, v28, v44\n"
    "v_fmac_f32 v55, v29, v45\n"
    "v_mfma_f32_16x16x32_bf16 v[20:23], v[172:175], v[236:239], v[20:23]\n"
    "v_fmac_f32 v54, v30, v46\n"
    "v_fmac_f32 v55, v31, v47\n"
    "v_mfma_f32_16x16x32_bf16 v[24:27], v[176:179], v[224:227], 0\n"
    "ds_read_b128 v[44:47], v56 offset:3072\n"
    "s_waitcnt lgkmcnt(2)\n"
    "v_fmac_f32 v52, v32, v48\n"
    "v_fmac_f32 v53, v33, v49\n"
    "v_mfma_f32_16x16x32_bf16 v[24:27], v[184:187], v[232:235], v[24:27]\n"
    "v_fmac_f32 v52, v34, v50\n"
    "v_fmac_f32 v53, v35, v51\n"
    "v_mfma_f32_16x16x32_bf16 v[28:31], v[180:183], v[228:231], 0\n"
    "v_fmac_f32 v54, v36, v48\n"
    "v_fmac_f32 v55, v37, v49\n"
    "v_mfma_f32_16x16x32_bf16 v[28:31], v[188:191], v[236:239], v[28:31]\n"
    "v_fmac_f32 v54, v38, v50\n"
    "v_fmac_f32 v55, v39, v51\n"
    "s_setprio 0\n"
    "v_mfma_f32_16x16x32_bf16 v[32:35], v[192:195], v[224:227], 0\n"
    "ds_read_b128 v[48:51], v56 offset:4096\n"
    "s_waitcnt lgkmcnt(2)\n"
    "v_fmac_f32 v52, v16, v40\n"
    "v_fmac_f32 v53, v17, v41\n"
    "v_mfma_f32_16x16x32_bf16 v[32:35], v[200:203], v[232:235], v[32:35]\n"
    "v_fmac_f32 v52, v18, v42\n"
    "v_fmac_f32 v53, v19, v43\n"
    "v_mfma_f32_16x16x32_bf16 v[36:39], v[196:199], v[228:231], 0\n"
    "v_fmac_f32 v54, v20, v40\n"
    "v_fmac_f32 v55, v21, v41\n"
    "v_mfma_f32_16x16x32_bf16 v[36:39], v[204:207], v[236:239], v[36:39]\n"
    "v_fmac_f32 v54, v22, v42\n"
    "v_fmac_f32 v55, v23, v43\n"
    "v_mfma_f32_16x16x32_bf16 v[16:19], v[208:211], v[224:227], 0\n"
    "ds_read_b128 v[40:43], v56 offset:5120\n"
    "s_waitcnt lgkmcnt(2)\n"
    "v_fmac_f32 v52, v24, v44\n"
    "v_fmac_f32 v53, v25, v45\n"
    "v_mfma_f32_16x16x32_bf16 v[16:19], v[216:219], v[232:235], v[16:19]\n"
    "v_fmac_f32 v52, v26, v46\n"
    "v_fmac_f32 v53, v27, v47\n"
    "v_mfma_f32_16x16x32_bf16 v[20:23], v[212:215], v[228:231], 0\n"
    "v_fmac_f32 v54, v28, v44\n"
    "v_fmac_f32 v55, v29, v45\n"
    "v_mfma_f32_16x16x32_bf16 v[20:23], v[220:223], v[236:239], v[20:23]\n"
    "v_fmac_f32 v54, v30, v46\n"
    "v_fmac_f32 v55, v31, v47\n"
    "v_mfma_f32_16x16x32_bf16 v[24:27], v[224:227], v[224:227], 0\n"
    "ds_read_b128 v[44:47], v56 offset:6144\n"
    "s_waitcnt lgkmcnt(2)\n"
    "v_fmac_f32 v52, v32, v48\n"
    "v_fmac_f32 v53, v33, v49\n"
    "v_mfma_f32_16x16x32_bf16 v[24:27], v[232:235], v[232:235], v[24:27]\n"
    "v_fmac_f32 v52, v34, v50\n"
    "v_fmac_f32 v53, v35, v51\n"
    "v_mfma_f32_16x16x32_bf16 v[28:31], v[228:231], v[228:231], 0\n"
    "v_fmac_f32 v54, v36, v48\n"
    "v_fmac_f32 v55, v37, v49\n"
    "v_mfma_f32_16x16x32_bf16 v[28:31], v[236:239], v[236:239], v[28:31]\n"
    "v_fmac_f32 v54, v38, v50\n"
    "v_fmac_f32 v55, v39, v51\n"
    "v_mfma_f32_16x16x32_bf16 v[32:35], v[128:131], v[208:211], 0\n"
    "ds_read_b32 v48, v58 offset:1024\n"
    "s_waitcnt lgkmcnt(2)\n"
    "v_fmac_f32 v52, v16, v40\n"
    "v_fmac_f32 v53, v17, v41\n"
    "v_mfma_f32_16x16x32_bf16 v[32:35], v[136:139], v[216:219], v[32:35]\n"
    "v_fmac_f32 v52, v18, v42\n"
    "v_fmac_f32 v53, v19, v43\n"
    "v_mfma_f32_16x16x32_bf16 v[36:39], v[132:135], v[212:215], 0\n"
    "v_fmac_f32 v54, v20, v40\n"
    "v_fmac_f32 v55, v21, v41\n"
    "v_mfma_f32_16x16x32_bf16 v[36:39], v[140:143], v[220:223], v[36:39]\n"
    "v_fmac_f32 v54, v22, v42\n"
    "v_fmac_f32 v55, v23, v43\n"
    "v_mfma_f32_16x16x32_bf16 v[16:19], v[128:131], v[224:227], 0\n"
    "ds_read_b32 v40, v58 offset:1280\n"
    "s_waitcnt lgkmcnt(2)\n"
    "v_fmac_f32 v52, v24, v44\n"
    "v_fmac_f32 v53, v25, v45\n"
    "v_mfma_f32_16x16x32_bf16 v[16:19], v[136:139], v[232:235], v[16:19]\n"
    "v_fmac_f32 v52, v26, v46\n"
    "v_fmac_f32 v53, v27, v47\n"
    "v_mfma_f32_16x16x32_bf16 v[20:23], v[132:135], v[228:231], 0\n"
    "v_fmac_f32 v54, v28, v44\n"
    "v_fmac_f32 v55, v29, v45\n"
    "v_mfma_f32_16x16x32_bf16 v[20:23], v[140:143], v[236:239], v[20:23]\n"
    "v_fmac_f32 v54, v30, v46\n"
    "v_fmac_f32 v55, v31, v47\n"
    "s_waitcnt vmcnt(0)\n"
    "v_cmp_ne_u32_e64 vcc, s12, v93\n"
    "v_cmp_ne_u32_e64 s[14:15], s13, v95\n"
    "s_waitcnt lgkmcnt(1)\n"
    "v_fmac_f32 v52, v32, v48\n"
    "v_fmac_f32 v54, v36, v48\n"
    "v_add_f32 v52, v52, v53\n"
    "v_add_f32 v54, v54, v55\n"
    "s_or_b64 s[14:15], s[14:15], vcc\n"
    "s_cbranch_scc1 L_p3aA_%=\n"
    "L_p3bA_%=:\n"
    "v_add_f32 v52, v92, v52\n"
    "v_add_f32 v54, v94, v54\n"
    "s_waitcnt lgkmcnt(0)\n"
    "v_fmac_f32 v52, v16, v40\n"
    "v_fmac_f32 v54, v20, v40\n"
    "s_nop 1\n"
    "v_permlane32_swap_b32 v52, v54\n"
    "v_add_f32 v52, v52, v54\n"
    "s_nop 1\n"
    "v_add_f32_dpp v52, v52, v52 row_ror:8 row_mask:0xf bank_mask:0xf\n"
    "s_nop 1\n"
    "v_add_f32_dpp v52, v52, v52 row_ror:4 row_mask:0xf bank_mask:0xf\n"
    "s_nop 1\n"
    "v_add_f32_dpp v52, v52, v52 row_ror:2 row_mask:0xf bank_mask:0xf\n"
    "s_nop 1\n"
    "v_add_f32_dpp v52, v52, v52 row_ror:1 row_mask:0xf bank_mask:0xf\n"
    "v_add_f32 v111, v52, v111\n"
    "s_mov_b32 s16, 0x10000\n"
    "s_mov_b32 s17, s16\n"
    "v_add_f32_dpp v111, v52, v111 row_bcast:15 row_mask:0xa bank_mask:0xf\n"
    "s_mov_b64 exec, s[16:17]\n"
    "v_cvt_pk_bf16_f32 v52, v111, v111\n"
    "global_store_short v110, v52, s[28:29]\n"
    "s_cmp_lg_u32 s53, 0\n"
    "s_cbranch_scc1 L_FIN_%=\n"
    "s_mov_b64 exec, 1\n"
    "v_mov_b32 v9, s12\n"
    "global_store_dword v110, v9, s[26:27]\n"
    "s_branch L_FIN_%=\n"
    "L_p3aA_%=:\n"
    "s_mov_b32 s66, 65536\n"
    "L_p3cA_%=:\n"
    "s_sleep 2\n"
    "s_mov_b64 exec, s[14:15]\n"
    "buffer_load_dwordx4 v[92:95], v108, s[48:51], s63 offen sc1\n"
    "s_mov_b64 exec, -1\n"
    "s_waitcnt vmcnt(0)\n"
    "v_cmp_ne_u32_e64 vcc, s12, v93\n"
    "v_cmp_ne_u32_e64 s[14:15], s13, v95\n"
    "s_nop 3\n"
    "s_or_b64 s[14:15], s[14:15], vcc\n"
    "s_cbranch_scc0 L_p3bA_%=\n"
    "s_sub_u32 s66, s66, 1\n"
    "s_cmp_lg_u32 s66, 0\n"
    "s_cbranch_scc1 L_p3cA_%=\n"
    "s_branch L_p3bA_%=\n"
    "L_FIN_%=:\n"
    "s_mov_b64 exec, -1\n"
    "L_ENDK_%=:\n"
    : "+{v0}"(tid), "+{v1}"(lds0)
    : [ka] "s"(__builtin_amdgcn_kernarg_segment_ptr()), [wg] "s"(blockIdx.x)
    : "memory", "vcc", "scc", "v2", "v3", "v4", "v5", "v6", "v7", "v8", "v9", "v10", "v11", "v12", "v13", "v14", "v15", "v16", "v17", "v18", "v19", "v20", "v21", "v22", "v23", "v24", "v25", "v26", "v27", "v28", "v29", "v30", "v31", "v32", "v33", "v34", "v35", "v36", "v37", "v38", "v39", "v40", "v41", "v42", "v43", "v44", "v45", "v46", "v47", "v48", "v49", "v50", "v51", "v52", "v53", "v54", "v55", "v56", "v57", "v58", "v59", "v60", "v61", "v62", "v63", "v64", "v65", "v66", "v67", "v68", "v69", "v70", "v71", "v72", "v73", "v74", "v75", "v76", "v77", "v78", "v79", "v80", "v81", "v82", "v83", "v84", "v85", "v86", "v87", "v88", "v89", "v90", "v91", "v92", "v93", "v94", "v95", "v96", "v97", "v98", "v99", "v100", "v101", "v102", "v103", "v104", "v105", "v106", "v107", "v108", "v109", "v110", "v111", "v112", "v113", "v114", "v115", "v116", "v117", "v118", "v119", "v120", "v121", "v122", "v123", "v124", "v125", "v126", "v127", "v128", "v129", "v130", "v131", "v132", "v133", "v134", "v135", "v136", "v137", "v138", "v139", "v140", "v141", "v142", "v143", "v144", "v145", "v146", "v147", "v148", "v149", "v150", "v151", "v152", "v153", "v154", "v155", "v156", "v157", "v158", "v159", "v160", "v161", "v162", "v163", "v164", "v165", "v166", "v167", "v168", "v169", "v170", "v171", "v172", "v173", "v174", "v175", "v176", "v177", "v178", "v179", "v180", "v181", "v182", "v183", "v184", "v185", "v186", "v187", "v188", "v189", "v190", "v191", "v192", "v193", "v194", "v195", "v196", "v197", "v198", "v199", "v200", "v201", "v202", "v203", "v204", "v205", "v206", "v207", "v208", "v209", "v210", "v211", "v212", "v213", "v214", "v215", "v216", "v217", "v218", "v219", "v220", "v221", "v222", "v223", "v224", "v225", "v226", "v227", "v228", "v229", "v230", "v231", "v232", "v233", "v234", "v235", "v236", "v237", "v238", "v239", "v240", "v241", "v242", "v243", "v244", "v245", "v246", "v247", "v248", "v249", "v250", "v251", "v252", "v253", "v254", "v255", "s4", "s5", "s6", "s7", "s10", "s11", "s12", "s13", "s14", "s15", "s16", "s17", "s18", "s19", "s20", "s21", "s22", "s23", "s24", "s25", "s26", "s27", "s28", "s29", "s30", "s31", "s33", "s34", "s35", "s36", "s37", "s38", "s39", "s40", "s41", "s42", "s43", "s44", "s45", "s46", "s47", "s48", "s49", "s50", "s51", "s52", "s53", "s54", "s55", "s56", "s57", "s58", "s59", "s60", "s61", "s62", "s63", "s64", "s65", "s66", "s67", "s68", "s69", "s70", "s71", "s72", "s73", "s74", "s75", "s76", "s77", "s78", "s79", "s80", "s81", "s82", "s83", "s84", "s85", "s86", "s87", "s88", "s89", "s90", "s91", "s92", "s93", "s94", "s95", "s96", "s97", "s98", "s99", "s100", "s101");
}

extern "C" void kernel_launch(void* const* d_in, const int* in_sizes, int n_in, void* d_out, int out_size, void* d_ws,
                              size_t ws_size, hipStream_t stream) {
  unsigned nreg = 1;
  while (nreg < 8 && WS_REG0 + 2 * nreg * WS_REGSZ <= ws_size) nreg *= 2;
  fused<<<256, 512, 0, stream>>>((const int*)d_in[0], (const unsigned short*)d_in[1], (const unsigned short*)d_in[2],
                                 (const unsigned short*)d_in[3], (const unsigned short*)d_in[4], (const unsigned short*)d_in[5],
                                 (const unsigned short*)d_in[6], (char*)d_ws, (unsigned short*)d_out,
                                 (unsigned long long)(nreg - 1));
}
```
